```python
import torch

def _softmax(x, axis=-1):
    return torch.softmax(x, dim=axis)
def _gelu(x):
    return torch.nn.functional.gelu(x, approximate="tanh")
def _silu(x):
    return torch.nn.functional.silu(x)
def _sigmoid(x):
    return torch.sigmoid(x)
def _layernorm(x, axis=-1, eps=1e-5):
    m = x.mean(dim=axis, keepdim=True)
    v = x.var(dim=axis, keepdim=True, unbiased=False)
    return (x - m) / torch.sqrt(v + eps)
def _scatter_add(tgt, idx, src, axis=0):
    return tgt.clone().index_add_(axis, idx, src)
def _segment_sum(src, seg, n):
    out = torch.zeros((n,) + src.shape[1:], dtype=src.dtype)
    return out.index_add_(0, seg, src)
def _pad(x, pw):
    flat = tuple(v for pair in reversed(pw) for v in pair)
    return torch.nn.functional.pad(x, flat)

kernel_name = 'interleave_layers__mha_block__mixer_channel_mlp__prompt'

def setup_inputs():

    _w = (lambda a: (lambda t: t.to(torch.bfloat16) if t.is_floating_point() else t)(torch.as_tensor(a)))
    def _A_setup():
        B, S, D, H = 1, 2048, 512, 32
        return {
            "x": _w((lambda *s: torch.randn(*s, dtype=torch.bfloat16) * 0.05)(B, S, D)),
            "wqkv": _w((lambda *s: torch.randn(*s, dtype=torch.bfloat16) * 0.05)(D, 3 * D)),
            "wo": _w((lambda *s: torch.randn(*s, dtype=torch.bfloat16) * 0.05)(D, D)),
        }
    _A_in = _A_setup()

    def _B_setup():
        B, S, D, F = 1, 2048, 512, 1792
        return {
            "x": _w((lambda *s: torch.randn(*s, dtype=torch.bfloat16) * 0.05)(B, S, D)),
            "w1": _w((lambda *s: torch.randn(*s, dtype=torch.bfloat16) * 3.2)(D, F) * (D ** -0.5)),
            "b1": _w((lambda *s: torch.randn(*s, dtype=torch.bfloat16) * 0.8)(F)),
            "w2": _w((lambda *s: torch.randn(*s, dtype=torch.bfloat16) * 0.2)(F, D) * (F ** -0.5)),
            "b2": _w((lambda *s: torch.randn(*s, dtype=torch.bfloat16) * 0.2)(D)),
        }
    _B_in = _B_setup()

    return {
        "A_x": _A_in["x"],
        "A_wqkv": _A_in["wqkv"],
        "A_wo": _A_in["wo"],
        "B_x": _B_in["x"],
        "B_w1": _B_in["w1"],
        "B_b1": _B_in["b1"],
        "B_w2": _B_in["w2"],
        "B_b2": _B_in["b2"],
    }

def reference(A_x, A_wqkv, A_wo, B_x, B_w1, B_b1, B_w2, B_b2):
    def _A_ref(x, wqkv, wo):
        B, S, D = x.shape
        H = 32
        Dh = D // H
        qkv = (lambda x, *d: x.permute(*d))((x @ wqkv).reshape(B, S, 3, H, Dh), 2, 0, 3, 1, 4)
        q, k, v = qkv[0], qkv[1], qkv[2]
        scores = torch.einsum("bhqd,bhkd->bhqk", q, k) * (Dh ** -0.5)
        p = _softmax(scores, axis=-1)
        o = torch.einsum("bhqk,bhkd->bhqd", p, v)
        return (lambda x, *d: x.permute(*d))(o, 0, 2, 1, 3).reshape(B, S, D) @ wo

    def _B_ref(x, w1, b1, w2, b2):
        y = _layernorm(x, axis=-1)
        h = _gelu(torch.einsum("bsd,df->bsf", y, w1) + b1)
        return torch.einsum("bsf,fd->bsd", h, w2) + b2

    x = A_x
    x = x + _A_ref(_layernorm(x, axis=-1), A_wqkv, A_wo)
    x = x + _B_ref(_layernorm(x, axis=-1), B_w1, B_b1, B_w2, B_b2)
    x = x + _A_ref(_layernorm(x, axis=-1), A_wqkv, A_wo)
    x = x + _B_ref(_layernorm(x, axis=-1), B_w1, B_b1, B_w2, B_b2)
    return x
```

```cpp
#include <hip/hip_runtime.h>
#include <hip/hip_bf16.h>
#include <stdint.h>

typedef unsigned short u16;
typedef __bf16 bf16x2 __attribute__((ext_vector_type(2)));
typedef __bf16 bf16x8 __attribute__((ext_vector_type(8)));
typedef __bf16 bf16x16 __attribute__((ext_vector_type(16)));
typedef float f32x2 __attribute__((ext_vector_type(2)));
typedef float f32x4 __attribute__((ext_vector_type(4)));
typedef float f32x8 __attribute__((ext_vector_type(8)));
typedef float f32x16 __attribute__((ext_vector_type(16)));
typedef unsigned u32x2 __attribute__((ext_vector_type(2)));
typedef unsigned u32x4 __attribute__((ext_vector_type(4)));
typedef short s16x4 __attribute__((ext_vector_type(4)));
#define LAS __attribute__((address_space(3)))
typedef LAS char* lds_ptr;

constexpr int SEQ = 2048, DM = 512, NH = 32, FF = 1792;
constexpr float LN_EPS = 1e-5f;
constexpr float QSCALE = 0.25f * 1.4426950408889634f;
constexpr float MAX_SAFE = 48.0f;

constexpr size_t MB = 1u << 20;
constexpr size_t WS_X0 = 0;
constexpr size_t WS_Q = 8 * MB;
constexpr size_t WS_K = 12 * MB;
constexpr size_t WS_V = 16 * MB;
constexpr size_t WS_O = 20 * MB;
constexpr size_t WS_H = 24 * MB;
constexpr size_t WS_ST = 40 * MB;
constexpr size_t WS_KN = 41 * MB;
constexpr size_t WS_CS = 41 * MB + 65536;

#define MFMA32(a, b, c) __builtin_amdgcn_mfma_f32_32x32x16_bf16(a, b, c, 0, 0, 0)
#define BAR() asm volatile("s_waitcnt lgkmcnt(0)\n\ts_barrier" ::: "memory")
#define WAITVM(n) asm volatile("s_waitcnt vmcnt(" #n ")" ::: "memory")

__device__ __forceinline__ void glds16(const void* g, unsigned lds_base) {
    unsigned sv;
    asm volatile("s_mov_b32 %0, m0\n\ts_mov_b32 m0, %2\n\ts_nop 0\n\tglobal_load_lds_dwordx4 %1, off\n\ts_mov_b32 m0, %0"
                 : "=&s"(sv) : "v"(g), "s"(lds_base) : "memory");
}
__device__ __forceinline__ unsigned pk2(float a, float b) {
    bf16x2 p; p[0] = (__bf16)a; p[1] = (__bf16)b; return __builtin_bit_cast(unsigned, p);
}
__device__ __forceinline__ float bflo(unsigned u) { return __uint_as_float(u << 16); }
__device__ __forceinline__ float bfhi(unsigned u) { return __uint_as_float(u & 0xffff0000u); }
__device__ __forceinline__ float swap32_add(float v) {
    auto r = __builtin_amdgcn_permlane32_swap(__float_as_uint(v), __float_as_uint(v), false, false);
    return __uint_as_float(r[0]) + __uint_as_float(r[1]);
}
__device__ __forceinline__ float swap32_max(float v) {
    auto r = __builtin_amdgcn_permlane32_swap(__float_as_uint(v), __float_as_uint(v), false, false);
    return fmaxf(__uint_as_float(r[0]), __uint_as_float(r[1]));
}
__device__ __forceinline__ u32x2 tr_read(lds_ptr p) {
    return __builtin_bit_cast(u32x2, __builtin_amdgcn_ds_read_tr16_b64_v4i16((LAS s16x4*)p));
}
__device__ __forceinline__ bf16x8 wfrag(lds_ptr p, int second_off) {
    u32x2 a = tr_read(p), b = tr_read(p + second_off);
    u32x4 r = {a[0], a[1], b[0], b[1]};
    return __builtin_bit_cast(bf16x8, r);
}
__device__ __forceinline__ bf16x8 ldg_frag(const char* p) { return __builtin_bit_cast(bf16x8, *(const u32x4*)p); }

__device__ __forceinline__ void row_stats(const char* st, int T, int lane, float& mean, float& var) {
    const int c = lane & 31, h = lane >> 5;
    const f32x2* p = (const f32x2*)(st + (size_t)T * 4096 + h * 2048 + c * 8);
    float s = 0.f, q = 0.f;
#pragma unroll
    for (int i = 0; i < 8; ++i) { f32x2 v = p[i * 32]; s += v[0]; q += v[1]; }
    s = swap32_add(s); q = swap32_add(q);
    mean = s * (1.0f / DM);
    var = fmaxf(q * (1.0f / DM) - mean * mean, 0.f);
}

__global__ __launch_bounds__(256) void k_pro(const u16* __restrict__ x0, const u16* __restrict__ wqkv, const u16* __restrict__ w1, char* __restrict__ ws) {
    __shared__ __attribute__((aligned(16))) char sm[32 * 272];
    __shared__ float red[4][64];
    const int tid = threadIdx.x, lane = tid & 63, w = tid >> 6, b = blockIdx.x;
    if (b < 256) {
        const int T = b >> 2, qd = b & 3;
#pragma unroll
        for (int i = 0; i < 2; ++i) {
            const int e = tid + i * 256, row = e >> 4, ch = e & 15;
            u32x4 v = *(const u32x4*)((const char*)x0 + (size_t)(T * 32 + row) * 1024 + qd * 256 + ch * 16);
            *(u32x4*)(sm + row * 272 + ch * 16) = v;
        }
        __syncthreads();
        const int c = lane & 31, h = lane >> 5;
        float s = 0.f, q = 0.f;
#pragma unroll
        for (int ks = 0; ks < 2; ++ks) {
            u32x2 a = *(const u32x2*)(sm + c * 272 + w * 64 + ks * 32 + h * 8);
            u32x2 d = *(const u32x2*)(sm + c * 272 + w * 64 + ks * 32 + 16 + h * 8);
            u32x4 v = {a[0], a[1], d[0], d[1]};
            *(u32x4*)(ws + WS_X0 + ((size_t)(T * 32 + qd * 8 + w * 2 + ks) * 64 + lane) * 16) = v;
#pragma unroll
            for (int j = 0; j < 4; ++j) { float lo = bflo(v[j]), hi = bfhi(v[j]); s += lo + hi; q += lo * lo + hi * hi; }
        }
        s = swap32_add(s); q = swap32_add(q);
        if (h == 0) { f32x2 o = {s, q}; *(f32x2*)(ws + WS_ST + (size_t)T * 4096 + (qd * 4 + w) * 256 + c * 8) = o; }
    } else {
        const int cb = b - 256;
        const u16* W; int N, n0, o0;
        if (cb < 24) { W = wqkv; N = 1536; n0 = cb * 64; o0 = n0; } else { W = w1; N = FF; n0 = (cb - 24) * 64; o0 = 1536 + n0; }
        float a[8] = {0, 0, 0, 0, 0, 0, 0, 0};
#pragma unroll
        for (int i = 0; i < 16; ++i) {
            const int row = w * 128 + i * 8 + (lane >> 3);
            u32x4 v = *(const u32x4*)(W + (size_t)row * N + n0 + (lane & 7) * 8);
#pragma unroll
            for (int j = 0; j < 4; ++j) { a[2 * j] += bflo(v[j]); a[2 * j + 1] += bfhi(v[j]); }
        }
#pragma unroll
        for (int j = 0; j < 8; ++j) { a[j] += __shfl_xor(a[j], 8); a[j] += __shfl_xor(a[j], 16); a[j] += __shfl_xor(a[j], 32); }
        if (lane < 8) {
#pragma unroll
            for (int j = 0; j < 8; ++j) red[w][lane * 8 + j] = a[j];
        }
        __syncthreads();
        if (tid < 64) ((float*)(ws + WS_CS))[o0 + tid] = red[0][tid] + red[1][tid] + red[2][tid] + red[3][tid];
    }
}

__global__ __launch_bounds__(512) void k_qkv(const u16* __restrict__ wqkv, char* __restrict__ ws, int layer) {
    extern __shared__ __attribute__((aligned(16))) char smem[];
    const int tid = threadIdx.x, lane = tid & 63, w = __builtin_amdgcn_readfirstlane(tid >> 6), b = blockIdx.x;
    const int rb = (b & 7) * 2 + (b >> 7), cb = (b >> 3) & 15;
    const unsigned lds0 = (unsigned)(uintptr_t)smem;
    if (w >= 4) {
        const int lw = w - 4;
#pragma unroll
        for (int c = 0; c < 4; ++c)
#pragma unroll
            for (int i = 0; i < 6; ++i) {
                const int ii = c * 24 + lw * 6 + i, e = ii * 64 + lane, k = e / 12, ch = e - k * 12;
                glds16((const char*)wqkv + (size_t)k * 3072 + cb * 192 + ch * 16, __builtin_amdgcn_readfirstlane(lds0 + ii * 1024));
            }
        WAITVM(18); BAR(); WAITVM(12); BAR(); WAITVM(6); BAR(); WAITVM(0); BAR();
        return;
    }
    const int T = rb * 4 + w, c = lane & 31, h = lane >> 5;
    const char* xs = ws + WS_X0 + (size_t)(layer * 2) * 2 * MB + (size_t)T * 32768 + lane * 16;
    bf16x8 xf[32];
#pragma unroll
    for (int s = 0; s < 32; ++s) xf[s] = ldg_frag(xs + s * 1024);
    float mean, var;
    row_stats(ws + WS_ST + (size_t)(layer * 2) * 262144, T, lane, mean, var);
    f32x16 acc[3];
#pragma unroll
    for (int a = 0; a < 3; ++a) acc[a] = f32x16{};
    lds_ptr wb = (lds_ptr)smem + (4 * h + ((lane & 15) >> 2)) * 192 + ((lane >> 4) & 1) * 32 + (lane & 3) * 8;
#pragma unroll
    for (int ch = 0; ch < 4; ++ch) {
        BAR();
#pragma unroll
        for (int s8 = 0; s8 < 8; ++s8) {
            const int s = ch * 8 + s8;
#pragma unroll
            for (int a = 0; a < 3; ++a) acc[a] = MFMA32(wfrag(wb + s * 3072 + a * 64, 8 * 192), xf[s], acc[a]);
        }
    }
    const float rstd = __builtin_amdgcn_rsqf(var + LN_EPS);
    lds_ptr tb = (lds_ptr)smem + 98304 + w * 2048;
#pragma unroll
    for (int a = 0; a < 3; ++a) {
        const int g = cb * 3 + a, type = g >> 4, hp = g & 15;
        const float* cs = (const float*)(ws + WS_CS) + g * 32 + 4 * h;
        const float sc = type == 0 ? rstd * QSCALE : rstd;
        const float ms = -mean * sc;
        unsigned pk[8];
#pragma unroll
        for (int q = 0; q < 4; ++q) {
            f32x4 cv = *(const f32x4*)(cs + 8 * q);
            float v0 = fmaf(acc[a][4 * q], sc, cv[0] * ms), v1 = fmaf(acc[a][4 * q + 1], sc, cv[1] * ms);
            float v2 = fmaf(acc[a][4 * q + 2], sc, cv[2] * ms), v3 = fmaf(acc[a][4 * q + 3], sc, cv[3] * ms);
            pk[2 * q] = pk2(v0, v1); pk[2 * q + 1] = pk2(v2, v3);
        }
        if (type < 2) {
            char* dst = ws + (type == 0 ? WS_Q : WS_K) + (size_t)layer * 2 * MB + ((size_t)(hp * 2) * 64 + T) * 1024 + lane * 16;
            *(u32x4*)dst = u32x4{pk[0], pk[1], pk[2], pk[3]};
            *(u32x4*)(dst + 65536) = u32x4{pk[4], pk[5], pk[6], pk[7]};
            if (type == 1) {
                float n0 = 0.f, n1 = 0.f;
#pragma unroll
                for (int j = 0; j < 4; ++j) {
                    float x = bflo(pk[j]), y = bfhi(pk[j]); n0 += x * x + y * y;
                    x = bflo(pk[4 + j]); y = bfhi(pk[4 + j]); n1 += x * x + y * y;
                }
                n0 = swap32_add(n0); n1 = swap32_add(n1);
                float m = h ? n1 : n0;
#pragma unroll
                for (int o = 1; o < 32; o <<= 1) m = fmaxf(m, __shfl_xor(m, o));
                if (c == 0) ((float*)(ws + WS_KN + layer * 8192))[(hp * 2 + h) * 64 + T] = m;
            }
        } else {
#pragma unroll
            for (int q = 0; q < 4; ++q) *(LAS u32x2*)(tb + c * 64 + q * 16 + h * 8) = u32x2{pk[2 * q], pk[2 * q + 1]};
            lds_ptr rp = tb + (8 * h + ((lane & 15) >> 2)) * 64 + ((lane >> 4) & 1) * 32 + (lane & 3) * 8;
            char* dst = ws + WS_V + (size_t)layer * 2 * MB + ((size_t)(hp * 2 + ((lane >> 4) & 1)) * 64 + T) * 1024 + h * 256 + (lane & 15) * 16;
#pragma unroll
            for (int s = 0; s < 2; ++s) {
                u32x2 lo = tr_read(rp + s * 1024), hi = tr_read(rp + s * 1024 + 256);
                *(u32x4*)(dst + s * 512) = u32x4{lo[0], lo[1], hi[0], hi[1]};
            }
        }
    }
}

constexpr int AT_V = 65536, AT_VS = 272 * 4;
__global__ __launch_bounds__(512) void k_attn(char* __restrict__ ws, int layer) {
    extern __shared__ __attribute__((aligned(16))) char smem[];
    const int tid = threadIdx.x, lane = tid & 63, w = __builtin_amdgcn_readfirstlane(tid >> 6), b = blockIdx.x;
    const int head = (b & 7) * 4 + (b >> 6), qb = (b >> 3) & 7;
    const int c = lane & 31, h = lane >> 5, T = qb * 8 + w;
    const char* kg = ws + WS_K + (size_t)layer * 2 * MB + (size_t)head * 65536 + w * 1024 + lane * 16;
    const char* vg = ws + WS_V + (size_t)layer * 2 * MB + (size_t)head * 65536 + w * 1024 + lane * 16;
    u32x4 kr[8], vr[8];
    kr[0] = *(const u32x4*)kg; vr[0] = *(const u32x4*)vg;
    const bf16x8 qf = ldg_frag(ws + WS_Q + (size_t)layer * 2 * MB + ((size_t)head * 64 + T) * 1024 + lane * 16);
    float kn = ((const float*)(ws + WS_KN + layer * 8192))[head * 64 + lane];
#pragma unroll
    for (int i = 1; i < 8; ++i) { kr[i] = *(const u32x4*)(kg + i * 8192); vr[i] = *(const u32x4*)(vg + i * 8192); }
    lds_ptr L = (lds_ptr)smem;
    if (tid < 256) *(LAS u32x4*)(L + AT_V + tid * 272 + 256) = u32x4{0x3f803f80u, 0x3f803f80u, 0x3f803f80u, 0x3f803f80u};
    lds_ptr kw = L + w * 1024 + lane * 16;
    lds_ptr vw = L + AT_V + w * AT_VS + (lane >> 4) * 272 + (lane & 15) * 16;
    *(LAS u32x4*)kw = kr[0]; *(LAS u32x4*)vw = vr[0];
#pragma unroll
    for (int o = 1; o < 64; o <<= 1) kn = fmaxf(kn, __shfl_xor(kn, o));
    float qn = 0.f;
    { u32x4 qu = __builtin_bit_cast(u32x4, qf);
#pragma unroll
      for (int j = 0; j < 4; ++j) { float x = bflo(qu[j]), y = bfhi(qu[j]); qn += x * x + y * y; } }
    qn = swap32_add(qn);
    float mref = __builtin_amdgcn_sqrtf(qn * kn) * 1.001f;
    const bool exact = !__all(mref <= MAX_SAFE);
    lds_ptr kp = L + lane * 16;
    lds_ptr vp = L + AT_V + h * 272 + (c < 16 ? c : 16) * 16;
    f32x16 o = f32x16{};
    f32x16 negm;
    auto tile = [&](int t) {
        bf16x8 kf = *(LAS bf16x8*)(kp + t * 1024);
        bf16x8 v0 = *(LAS bf16x8*)(vp + t * AT_VS), v1 = *(LAS bf16x8*)(vp + t * AT_VS + 544);
        f32x16 s = MFMA32(kf, qf, negm);
        f32x8 p0, p1;
#pragma unroll
        for (int r = 0; r < 8; ++r) { p0[r] = __builtin_amdgcn_exp2f(s[r]); p1[r] = __builtin_amdgcn_exp2f(s[8 + r]); }
        o = __builtin_amdgcn_smfmac_f32_32x32x32_bf16(v0, __builtin_bit_cast(bf16x16, p0), o, 0xDDDD, 0, 0);
        o = __builtin_amdgcn_smfmac_f32_32x32x32_bf16(v1, __builtin_bit_cast(bf16x16, p1), o, 0xDDDD, 0, 0);
    };
    BAR();
    if (!exact) {
#pragma unroll
        for (int r = 0; r < 16; ++r) negm[r] = -mref;
#pragma unroll
        for (int t = 0; t < 8; ++t) tile(t);
    }
#pragma unroll
    for (int i = 1; i < 8; ++i) { *(LAS u32x4*)(kw + i * 8192) = kr[i]; *(LAS u32x4*)(vw + i * 8 * AT_VS) = vr[i]; }
    BAR();
    int t0 = 8;
    if (exact) {
        float m = -INFINITY;
        for (int t = 0; t < 64; ++t) {
            bf16x8 kf = *(LAS bf16x8*)(kp + t * 1024);
            f32x16 s = MFMA32(kf, qf, f32x16{});
#pragma unroll
            for (int r = 0; r < 16; ++r) m = fmaxf(m, s[r]);
        }
        m = swap32_max(m);
#pragma unroll
        for (int r = 0; r < 16; ++r) negm[r] = -m;
        t0 = 0;
    }
    for (int t = t0; t < 64; t += 8) {
#pragma unroll
        for (int u = 0; u < 8; ++u) tile(t + u);
    }
    const float inv = __builtin_amdgcn_rcpf(o[8]);
    u32x4 ov = {pk2(o[0] * inv, o[1] * inv), pk2(o[2] * inv, o[3] * inv), pk2(o[4] * inv, o[5] * inv), pk2(o[6] * inv, o[7] * inv)};
    *(u32x4*)(ws + WS_O + (size_t)layer * 2 * MB + ((size_t)T * 32 + head) * 1024 + lane * 16) = ov;
}

template <int KS, bool BIAS, bool FINAL>
__global__ __launch_bounds__(512) void k_down(const u16* __restrict__ W, const u16* __restrict__ bias, const char* __restrict__ A,
                                              const char* __restrict__ xres, char* __restrict__ xout, char* __restrict__ stout) {
    extern __shared__ __attribute__((aligned(16))) char smem[];
    constexpr int NCH = KS / 8, NSLOT = NCH < 4 ? NCH : 4;
    const int tid = threadIdx.x, lane = tid & 63, w = __builtin_amdgcn_readfirstlane(tid >> 6), b = blockIdx.x;
    const int rb = (b & 7) * 4 + (b >> 6), cb = (b >> 3) & 7;
    const unsigned lds0 = (unsigned)(uintptr_t)smem;
    if (w >= 4) {
        const int lw = w - 4;
        const int r8 = lane >> 3;
        const char* src = (const char*)W + (size_t)((lw >> 1) * KS * 16 + (lw & 1) * 64 + r8) * 1024 + cb * 128 + (((lane & 7) ^ (((r8 >> 1) & 1) << 2)) * 16);
        auto issue = [&](int ch) {
#pragma unroll
            for (int i = 0; i < 8; ++i)
                glds16(src + (size_t)(ch * 128 + i * 8) * 1024, __builtin_amdgcn_readfirstlane(lds0 + (ch % NSLOT) * 32768 + lw * 8192 + i * 1024));
        };
#pragma unroll
        for (int ch = 0; ch < NSLOT; ++ch) issue(ch);
#pragma unroll
        for (int ch = 0; ch < NCH; ++ch) {
            const int issued = (NSLOT + (ch > 0 ? ch - 1 : 0)) < NCH ? (NSLOT + (ch > 0 ? ch - 1 : 0)) : NCH;
            const int left = issued - ch - 1;
            if (left == 3) WAITVM(24); else if (left == 2) WAITVM(16); else if (left == 1) WAITVM(8); else WAITVM(0);
            BAR();
            if (ch >= 1 && NSLOT + ch - 1 < NCH) issue(NSLOT + ch - 1);
        }
        BAR(); BAR();
        return;
    }
    const int tt = w & 1, kh = w >> 1, T = rb * 2 + tt, c = lane & 31, h = lane >> 5;
    constexpr int PF = KS < 24 ? KS : 24;
    const char* as = A + ((size_t)T * (2 * KS) + kh * KS) * 1024 + lane * 16;
    bf16x8 af[PF];
#pragma unroll
    for (int s = 0; s < PF; ++s) af[s] = ldg_frag(as + s * 1024);
    const int strip = cb * 2 + kh;
    const char* xr = xres + ((size_t)T * 32 + strip * 2) * 1024 + lane * 16;
    const u32x4 r0 = *(const u32x4*)xr, r1 = *(const u32x4*)(xr + 1024);
    u32x2 bv[4];
    if (BIAS) {
#pragma unroll
        for (int q = 0; q < 4; ++q) bv[q] = *(const u32x2*)(bias + strip * 32 + 8 * q + 4 * h);
    }
    f32x16 acc0 = f32x16{}, acc1 = f32x16{};
    const int q4 = (lane & 15) >> 2;
    lds_ptr wb = (lds_ptr)smem + kh * 16384 + (4 * h + q4) * 128 + ((lane >> 4) & 1) * 32 + (lane & 3) * 8;
    lds_ptr wb0 = wb + ((kh ^ (q4 >> 1)) * 64), wb1 = wb + ((kh ^ 1 ^ (q4 >> 1)) * 64);
#pragma unroll
    for (int ch = 0; ch < NCH; ++ch) {
        BAR();
#pragma unroll
        for (int s8 = 0; s8 < 8; ++s8) {
            const int s = ch * 8 + s8, off = (ch % NSLOT) * 32768 + s8 * 2048;
            const bf16x8 a = af[s % PF];
            acc0 = MFMA32(wfrag(wb0 + off, 1024), a, acc0);
            acc1 = MFMA32(wfrag(wb1 + off, 1024), a, acc1);
            if (s + PF < KS) af[s % PF] = ldg_frag(as + (s + PF) * 1024);
        }
    }
    BAR();
    {
        lds_ptr ex = (lds_ptr)smem + w * 4096 + lane * 16;
#pragma unroll
        for (int q = 0; q < 4; ++q) *(LAS f32x4*)(ex + q * 1024) = f32x4{acc1[4 * q], acc1[4 * q + 1], acc1[4 * q + 2], acc1[4 * q + 3]};
        BAR();
        lds_ptr ey = (lds_ptr)smem + (w ^ 2) * 4096 + lane * 16;
#pragma unroll
        for (int q = 0; q < 4; ++q) { f32x4 v = *(LAS f32x4*)(ey + q * 1024); acc0[4 * q] += v[0]; acc0[4 * q + 1] += v[1]; acc0[4 * q + 2] += v[2]; acc0[4 * q + 3] += v[3]; }
    }
    float s = 0.f, sq = 0.f;
    unsigned pk[8];
#pragma unroll
    for (int j = 0; j < 8; ++j) {
        const unsigned ru = j < 4 ? r0[j & 3] : r1[j & 3];
        float a0 = acc0[2 * j], a1 = acc0[2 * j + 1];
        if (BIAS) { const unsigned bu = bv[j >> 1][j & 1]; a0 += bflo(bu); a1 += bfhi(bu); }
        a0 += bflo(ru); a1 += bfhi(ru);
        pk[j] = pk2(a0, a1);
        a0 = bflo(pk[j]); a1 = bfhi(pk[j]);
        s += a0 + a1; sq += a0 * a0 + a1 * a1;
    }
    if (FINAL) {
        char* dst = xout + (size_t)(T * 32 + c) * 1024 + strip * 64 + h * 8;
#pragma unroll
        for (int q = 0; q < 4; ++q) *(u32x2*)(dst + q * 16) = u32x2{pk[2 * q], pk[2 * q + 1]};
    } else {
        char* dst = xout + ((size_t)T * 32 + strip * 2) * 1024 + lane * 16;
        *(u32x4*)dst = u32x4{pk[0], pk[1], pk[2], pk[3]};
        *(u32x4*)(dst + 1024) = u32x4{pk[4], pk[5], pk[6], pk[7]};
        s = swap32_add(s); sq = swap32_add(sq);
        if (h == 0) *(f32x2*)(stout + (size_t)T * 4096 + strip * 256 + c * 8) = f32x2{s, sq};
    }
}

__global__ __launch_bounds__(512) void k_up(const u16* __restrict__ w1, const u16* __restrict__ b1, char* __restrict__ ws, int layer) {
    extern __shared__ __attribute__((aligned(16))) char smem[];
    const int tid = threadIdx.x, lane = tid & 63, w = __builtin_amdgcn_readfirstlane(tid >> 6), b = blockIdx.x;
    const int i28 = b >> 3, rb = (b & 7) * 2 + (i28 >= 14), cb = i28 >= 14 ? i28 - 14 : i28;
    const unsigned lds0 = (unsigned)(uintptr_t)smem;
    if (w >= 4) {
        const int lw = w - 4, r4 = lane >> 4;
        const char* src = (const char*)w1 + (size_t)(lw * 32 + r4) * (FF * 2) + cb * 256 + (((lane & 15) ^ (r4 << 2)) * 16);
#pragma unroll
        for (int ch = 0; ch < 4; ++ch)
#pragma unroll
            for (int i = 0; i < 8; ++i)
                glds16(src + (size_t)(ch * 128 + i * 4) * (FF * 2), __builtin_amdgcn_readfirstlane(lds0 + ch * 32768 + lw * 8192 + i * 1024));
        WAITVM(24); BAR(); WAITVM(16); BAR(); WAITVM(8); BAR(); WAITVM(0); BAR();
        return;
    }
    const int T = rb * 4 + w, h = lane >> 5;
    const char* xs = ws + WS_X0 + (size_t)(layer * 2 + 1) * 2 * MB + (size_t)T * 32768 + lane * 16;
    bf16x8 xf[32];
#pragma unroll
    for (int s = 0; s < 32; ++s) xf[s] = ldg_frag(xs + s * 1024);
    float mean, var;
    row_stats(ws + WS_ST + (size_t)(layer * 2 + 1) * 262144, T, lane, mean, var);
    f32x16 acc[4];
#pragma unroll
    for (int a = 0; a < 4; ++a) acc[a] = f32x16{};
    const int q4 = (lane & 15) >> 2;
    lds_ptr wb = (lds_ptr)smem + (4 * h + q4) * 256 + ((lane >> 4) & 1) * 32 + (lane & 3) * 8;
#pragma unroll
    for (int ch = 0; ch < 4; ++ch) {
        BAR();
#pragma unroll
        for (int s8 = 0; s8 < 8; ++s8) {
            const int s = ch * 8 + s8;
#pragma unroll
            for (int a = 0; a < 4; ++a) acc[a] = MFMA32(wfrag(wb + ((a ^ q4) * 64) + s * 4096, 2048), xf[s], acc[a]);
        }
    }
    const float r1 = __builtin_amdgcn_rsqf(var + LN_EPS);
    const float sc = r1 * __builtin_amdgcn_rsqf(var * r1 * r1 + LN_EPS);
    const float ms = -mean * sc;
    constexpr float GA = -2.0f * 0.7978845608028654f * 1.4426950408889634f, GB = GA * 0.044715f;
#pragma unroll
    for (int a = 0; a < 4; ++a) {
        const int g = cb * 4 + a;
        const float* cs = (const float*)(ws + WS_CS) + 1536 + g * 32 + 4 * h;
        unsigned pk[8];
#pragma unroll
        for (int q = 0; q < 4; ++q) {
            const f32x4 cv = *(const f32x4*)(cs + 8 * q);
            const u32x2 bu = *(const u32x2*)(b1 + g * 32 + 8 * q + 4 * h);
            const float bb[4] = {bflo(bu[0]), bfhi(bu[0]), bflo(bu[1]), bfhi(bu[1])};
            float y[4];
#pragma unroll
            for (int j = 0; j < 4; ++j) {
                const float u = fmaf(acc[a][4 * q + j], sc, fmaf(cv[j], ms, bb[j]));
                const float e = __builtin_amdgcn_exp2f(u * fmaf(u * u, GB, GA));
                y[j] = u * __builtin_amdgcn_rcpf(1.0f + e);
            }
            pk[2 * q] = pk2(y[0], y[1]); pk[2 * q + 1] = pk2(y[2], y[3]);
        }
        char* dst = ws + WS_H + (size_t)layer * 8 * MB + ((size_t)T * 112 + g * 2) * 1024 + lane * 16;
        *(u32x4*)dst = u32x4{pk[0], pk[1], pk[2], pk[3]};
        *(u32x4*)(dst + 1024) = u32x4{pk[4], pk[5], pk[6], pk[7]};
    }
}

extern "C" void kernel_launch(void* const* d_in, const int* in_sizes, int n_in, void* d_out, int out_size, void* d_ws, size_t ws_size,
                              hipStream_t stream) {
    const u16* x0 = (const u16*)d_in[0];
    const u16* wqkv = (const u16*)d_in[1];
    const u16* wo = (const u16*)d_in[2];
    const u16* w1 = (const u16*)d_in[4];
    const u16* b1 = (const u16*)d_in[5];
    const u16* w2 = (const u16*)d_in[6];
    const u16* b2 = (const u16*)d_in[7];
    char* ws = (char*)d_ws;
    (void)hipFuncSetAttribute((const void*)k_qkv, hipFuncAttributeMaxDynamicSharedMemorySize, 106496);
    (void)hipFuncSetAttribute((const void*)k_attn, hipFuncAttributeMaxDynamicSharedMemorySize, AT_V + 64 * AT_VS);
    (void)hipFuncSetAttribute((const void*)k_down<16, false, false>, hipFuncAttributeMaxDynamicSharedMemorySize, 65536);
    (void)hipFuncSetAttribute((const void*)k_down<56, true, false>, hipFuncAttributeMaxDynamicSharedMemorySize, 131072);
    (void)hipFuncSetAttribute((const void*)k_down<56, true, true>, hipFuncAttributeMaxDynamicSharedMemorySize, 131072);
    (void)hipFuncSetAttribute((const void*)k_up, hipFuncAttributeMaxDynamicSharedMemorySize, 131072);
    k_pro<<<308, 256, 0, stream>>>(x0, wqkv, w1, ws);
    for (int l = 0; l < 2; ++l) {
        char* xin = ws + WS_X0 + (size_t)(2 * l) * 2 * MB;
        char* x1 = xin + 2 * MB;
        char* x2 = x1 + 2 * MB;
        k_qkv<<<256, 512, 106496, stream>>>(wqkv, ws, l);
        k_attn<<<256, 512, AT_V + 64 * AT_VS, stream>>>(ws, l);
        k_down<16, false, false><<<256, 512, 65536, stream>>>(wo, nullptr, ws + WS_O + (size_t)l * 2 * MB, xin, x1, ws + WS_ST + (size_t)(2 * l + 1) * 262144);
        k_up<<<224, 512, 131072, stream>>>(w1, b1, ws, l);
        if (l == 0) k_down<56, true, false><<<256, 512, 131072, stream>>>(w2, b2, ws + WS_H, x1, x2, ws + WS_ST + (size_t)2 * 262144);
        else k_down<56, true, true><<<256, 512, 131072, stream>>>(w2, b2, ws + WS_H + 8 * MB, x1, (char*)d_out, nullptr);
    }
}
```

```cpp
#include <hip/hip_runtime.h>
#include <hip/hip_bf16.h>
#include <stdint.h>


typedef unsigned short u16;
typedef unsigned long long u64;
typedef __bf16 bf16x2 __attribute__((ext_vector_type(2)));
typedef __bf16 bf16x8 __attribute__((ext_vector_type(8)));
typedef float f32x2 __attribute__((ext_vector_type(2)));
typedef float f32x4 __attribute__((ext_vector_type(4)));
typedef float f32x16 __attribute__((ext_vector_type(16)));
typedef unsigned u32x2 __attribute__((ext_vector_type(2)));
typedef unsigned u32x4 __attribute__((ext_vector_type(4)));
typedef short s16x4 __attribute__((ext_vector_type(4)));
#define LAS __attribute__((address_space(3)))
#define GAS __attribute__((address_space(1)))
typedef LAS char* lds_ptr;
typedef __amdgpu_buffer_rsrc_t rsrc_t;

constexpr int DM = 512, FF = 1792;
constexpr unsigned SPIN_CAP = 1u << 18;
constexpr float LN_EPS = 1e-5f;
constexpr float QSCALE = 0.25f * 1.4426950408889634f;
constexpr float MAX_SAFE = 48.0f;
constexpr int CODE_KB = 40;

constexpr unsigned MB = 1u << 20;
constexpr unsigned WS_EPOCH = 0;
constexpr unsigned WS_FLAG0 = 128 * 1024;
constexpr unsigned WS_ST0 = MB / 2, WS_X0 = 1 * MB;
constexpr unsigned WS_ARENA = 4 * MB, ARENA = 24 * MB, PAGE = 4 * MB;
constexpr unsigned A_FLAG = 0;
constexpr unsigned A_ST = 384 * 1024;
constexpr unsigned A_PROBE = 896 * 1024;
constexpr unsigned A_XLO = 1 * MB, A_XHI = PAGE;
constexpr unsigned A_O = PAGE + 1 * MB;
constexpr unsigned A_Q = 2 * PAGE, A_K = A_Q + MB / 2, A_V = A_Q + MB;
constexpr unsigned A_H = 4 * PAGE;
constexpr unsigned WS_END = WS_ARENA + 8 * ARENA;
__device__ __forceinline__ unsigned arena(int g, unsigned sides) { return WS_ARENA + g * ARENA + (((sides >> g) & 1) << 13); }
__device__ __forceinline__ constexpr unsigned stripe(unsigned o) { return ((o >> 13) << 14) | (o & 8191); }
__device__ __forceinline__ unsigned stripe_p(unsigned o, unsigned pitch) { return (o >> 13) * pitch + (o & 8191); }
__device__ __forceinline__ unsigned x_off(int xi, int T, unsigned sides) { return arena(T >> 3, sides) + (xi < 2 ? A_XLO : A_XHI) + (xi & 1) * (MB / 2) + (T & 7) * 65536; }
__device__ __forceinline__ unsigned st_off(int xi, int T, unsigned sides) { return arena(T >> 3, sides) + A_ST + xi * 65536 + stripe((T & 7) * 4096); }
__device__ __forceinline__ unsigned o_off(int layer, int T, unsigned sides) { return arena(T >> 3, sides) + A_O + layer * (MB / 2) + (T & 7) * 65536; }
__device__ __forceinline__ unsigned h_off(int layer, int T, unsigned sides) { return arena(T >> 3, sides) + A_H + layer * PAGE + (T & 7) * (14 * 16384); }
__device__ __forceinline__ unsigned qkv_off(int layer, int head, unsigned sides) { return arena(head >> 2, sides) + layer * PAGE + (head & 3) * 131072; }

constexpr int L_EX = 131072;
constexpr int L_TAB = L_EX + 24576;
constexpr int T_FQ = L_TAB, T_FU = L_TAB + 1536, T_KN = L_TAB + 3584, T_PART = L_TAB + 4096;
constexpr int LDS_BYTES = L_TAB + 6144;

#define MFMA32(a, b, c) __builtin_amdgcn_mfma_f32_32x32x16_bf16(a, b, c, 0, 0, 0)
#define MFMA_ASM0(acc, a, b) asm volatile("v_mfma_f32_32x32x16_bf16 %0, %1, %2, 0" : "=&v"(acc) : "v"(a), "v"(b) : "memory")
#define MFMA_ASM(acc, a, b) asm volatile("v_mfma_f32_32x32x16_bf16 %0, %1, %2, %0" : "+v"(acc) : "v"(a), "v"(b) : "memory")
#define MFMA_DONE2(a, b) asm volatile("s_nop 12" : "+v"(a), "+v"(b) :: "memory")
#define MFMA_DONE3(a, b, c) asm volatile("s_nop 12" : "+v"(a), "+v"(b), "+v"(c) :: "memory")
#define BAR() asm volatile("s_waitcnt lgkmcnt(0)\n\ts_barrier" ::: "memory")
#define BAR_RAW() asm volatile("s_barrier" ::: "memory")
#define DRAIN_BAR() asm volatile("s_waitcnt vmcnt(0) lgkmcnt(0)\n\ts_barrier" ::: "memory")
#define WAITVM(n) asm volatile("s_waitcnt vmcnt(" #n ")" ::: "memory")
#define PACE() WAITVM(8)
#define KEEP2(a, b) asm volatile("s_nop 1" :: "v"(a), "v"(b) : "memory")
#define PIN(x) asm volatile("" : "+v"(x))
#define OPAQUE_S(x) asm volatile("" : "+s"(x))

#define ATTN_LOOP_ASM \
    "s_nop 1\n\t" \
    "v_mfma_f32_32x32x16_bf16 v[168:183], v[200:203], %[q0], %[m0]\n\t" \
    "v_mov_b32 v240, %[vc]\n\t" \
    "v_mov_b32 v244, %[vc]\n\t" \
    "v_mov_b32 v241, %[vc]\n\t" \
    "v_mov_b32 v245, %[vc]\n\t" \
    "v_mov_b32 v242, %[vc]\n\t" \
    "v_mov_b32 v246, %[vc]\n\t" \
    "v_mov_b32 v243, %[vc]\n\t" \
    "v_mov_b32 v247, %[vc]\n\t" \
    "v_mov_b32 v192, 0\n\t" \
    "v_mov_b32 v193, 0\n\t" \
    "v_mov_b32 v194, 0\n\t" \
    "v_mov_b32 v195, 0\n\t" \
    "v_mov_b32 v196, 0\n\t" \
    "v_mov_b32 v197, 0\n\t" \
    "v_mov_b32 v198, 0\n\t" \
    "v_mov_b32 v199, 0\n\t" \
    "1:\n\t" \
    "v_exp_f32 v168, v168\n\t" \
    "v_exp_f32 v169, v169\n\t" \
    "v_smfmac_f32_32x32x32_bf16 %[o3], v[244:247], v[192:199], %[idx]\n\t" \
    "buffer_load_dwordx4 v[236:239], %[gk], %[rs], %[ko] offen offset:0 sc1\n\t" \
    "v_exp_f32 v170, v170\n\t" \
    "v_exp_f32 v171, v171\n\t" \
    "v_exp_f32 v172, v172\n\t" \
    "v_mfma_f32_32x32x16_bf16 v[184:199], v[200:203], %[q1], %[m1]\n\t" \
    "v_exp_f32 v173, v173\n\t" \
    "v_exp_f32 v174, v174\n\t" \
    "v_exp_f32 v175, v175\n\t" \
    "v_exp_f32 v176, v176\n\t" \
    "v_exp_f32 v177, v177\n\t" \
    "v_smfmac_f32_32x32x32_bf16 %[o0], v[204:207], v[168:175], %[idx]\n\t" \
    "s_mov_b64 exec, %[vm]\n\t" \
    "buffer_load_dwordx4 v[240:243], %[gv], %[rs], %[ko] offen offset:0 sc1\n\t" \
    "buffer_load_dwordx4 v[244:247], %[gv], %[rs], %[ko] offen offset:512 sc1\n\t" \
    "s_mov_b64 exec, -1\n\t" \
    "v_exp_f32 v178, v178\n\t" \
    "v_exp_f32 v179, v179\n\t" \
    "v_exp_f32 v180, v180\n\t" \
    "v_exp_f32 v181, v181\n\t" \
    "v_exp_f32 v182, v182\n\t" \
    "v_exp_f32 v183, v183\n\t" \
    "v_exp_f32 v184, v184\n\t" \
    "v_exp_f32 v185, v185\n\t" \
    "v_smfmac_f32_32x32x32_bf16 %[o0], v[208:211], v[176:183], %[idx]\n\t" \
    "v_exp_f32 v186, v186\n\t" \
    "v_exp_f32 v187, v187\n\t" \
    "v_exp_f32 v188, v188\n\t" \
    "v_mfma_f32_32x32x16_bf16 v[168:183], v[200:203], %[q2], %[m2]\n\t" \
    "v_exp_f32 v189, v189\n\t" \
    "v_exp_f32 v190, v190\n\t" \
    "v_exp_f32 v191, v191\n\t" \
    "v_exp_f32 v192, v192\n\t" \
    "v_exp_f32 v193, v193\n\t" \
    "v_smfmac_f32_32x32x32_bf16 %[o1], v[204:207], v[184:191], %[idx]\n\t" \
    "v_exp_f32 v194, v194\n\t" \
    "v_exp_f32 v195, v195\n\t" \
    "v_exp_f32 v196, v196\n\t" \
    "v_exp_f32 v197, v197\n\t" \
    "v_exp_f32 v198, v198\n\t" \
    "v_exp_f32 v199, v199\n\t" \
    "v_exp_f32 v168, v168\n\t" \
    "v_exp_f32 v169, v169\n\t" \
    "v_smfmac_f32_32x32x32_bf16 %[o1], v[208:211], v[192:199], %[idx]\n\t" \
    "v_exp_f32 v170, v170\n\t" \
    "v_exp_f32 v171, v171\n\t" \
    "v_exp_f32 v172, v172\n\t" \
    "v_mfma_f32_32x32x16_bf16 v[184:199], v[200:203], %[q3], %[m3]\n\t" \
    "v_exp_f32 v173, v173\n\t" \
    "v_exp_f32 v174, v174\n\t" \
    "v_exp_f32 v175, v175\n\t" \
    "v_exp_f32 v176, v176\n\t" \
    "v_exp_f32 v177, v177\n\t" \
    "v_smfmac_f32_32x32x32_bf16 %[o2], v[204:207], v[168:175], %[idx]\n\t" \
    "v_exp_f32 v178, v178\n\t" \
    "v_exp_f32 v179, v179\n\t" \
    "v_exp_f32 v180, v180\n\t" \
    "v_exp_f32 v181, v181\n\t" \
    "v_exp_f32 v182, v182\n\t" \
    "v_exp_f32 v183, v183\n\t" \
    "v_exp_f32 v184, v184\n\t" \
    "v_exp_f32 v185, v185\n\t" \
    "v_smfmac_f32_32x32x32_bf16 %[o2], v[208:211], v[176:183], %[idx]\n\t" \
    "v_exp_f32 v186, v186\n\t" \
    "v_exp_f32 v187, v187\n\t" \
    "s_waitcnt vmcnt(6)\n\t" \
    "v_exp_f32 v188, v188\n\t" \
    "v_mfma_f32_32x32x16_bf16 v[168:183], v[212:215], %[q0], %[m0]\n\t" \
    "v_exp_f32 v189, v189\n\t" \
    "v_exp_f32 v190, v190\n\t" \
    "v_exp_f32 v191, v191\n\t" \
    "v_exp_f32 v192, v192\n\t" \
    "v_exp_f32 v193, v193\n\t" \
    "v_smfmac_f32_32x32x32_bf16 %[o3], v[204:207], v[184:191], %[idx]\n\t" \
    "v_exp_f32 v194, v194\n\t" \
    "v_exp_f32 v195, v195\n\t" \
    "v_exp_f32 v196, v196\n\t" \
    "v_exp_f32 v197, v197\n\t" \
    "v_exp_f32 v198, v198\n\t" \
    "v_exp_f32 v199, v199\n\t" \
    "v_exp_f32 v168, v168\n\t" \
    "v_exp_f32 v169, v169\n\t" \
    "v_smfmac_f32_32x32x32_bf16 %[o3], v[208:211], v[192:199], %[idx]\n\t" \
    "buffer_load_dwordx4 v[200:203], %[gk], %[rs], %[ko] offen offset:1024 sc1\n\t" \
    "v_exp_f32 v170, v170\n\t" \
    "v_exp_f32 v171, v171\n\t" \
    "v_exp_f32 v172, v172\n\t" \
    "v_mfma_f32_32x32x16_bf16 v[184:199], v[212:215], %[q1], %[m1]\n\t" \
    "v_exp_f32 v173, v173\n\t" \
    "v_exp_f32 v174, v174\n\t" \
    "v_exp_f32 v175, v175\n\t" \
    "v_exp_f32 v176, v176\n\t" \
    "v_exp_f32 v177, v177\n\t" \
    "v_smfmac_f32_32x32x32_bf16 %[o0], v[216:219], v[168:175], %[idx]\n\t" \
    "s_mov_b64 exec, %[vm]\n\t" \
    "buffer_load_dwordx4 v[204:207], %[gv], %[rs], %[ko] offen offset:1024 sc1\n\t" \
    "buffer_load_dwordx4 v[208:211], %[gv], %[rs], %[ko] offen offset:1536 sc1\n\t" \
    "s_mov_b64 exec, -1\n\t" \
    "v_exp_f32 v178, v178\n\t" \
    "v_exp_f32 v179, v179\n\t" \
    "v_exp_f32 v180, v180\n\t" \
    "v_exp_f32 v181, v181\n\t" \
    "v_exp_f32 v182, v182\n\t" \
    "v_exp_f32 v183, v183\n\t" \
    "v_exp_f32 v184, v184\n\t" \
    "v_exp_f32 v185, v185\n\t" \
    "v_smfmac_f32_32x32x32_bf16 %[o0], v[220:223], v[176:183], %[idx]\n\t" \
    "v_exp_f32 v186, v186\n\t" \
    "v_exp_f32 v187, v187\n\t" \
    "v_exp_f32 v188, v188\n\t" \
    "v_mfma_f32_32x32x16_bf16 v[168:183], v[212:215], %[q2], %[m2]\n\t" \
    "v_exp_f32 v189, v189\n\t" \
    "v_exp_f32 v190, v190\n\t" \
    "v_exp_f32 v191, v191\n\t" \
    "v_exp_f32 v192, v192\n\t" \
    "v_exp_f32 v193, v193\n\t" \
    "v_smfmac_f32_32x32x32_bf16 %[o1], v[216:219], v[184:191], %[idx]\n\t" \
    "v_exp_f32 v194, v194\n\t" \
    "v_exp_f32 v195, v195\n\t" \
    "v_exp_f32 v196, v196\n\t" \
    "v_exp_f32 v197, v197\n\t" \
    "v_exp_f32 v198, v198\n\t" \
    "v_exp_f32 v199, v199\n\t" \
    "v_exp_f32 v168, v168\n\t" \
    "v_exp_f32 v169, v169\n\t" \
    "v_smfmac_f32_32x32x32_bf16 %[o1], v[220:223], v[192:199], %[idx]\n\t" \
    "v_exp_f32 v170, v170\n\t" \
    "v_exp_f32 v171, v171\n\t" \
    "v_exp_f32 v172, v172\n\t" \
    "v_mfma_f32_32x32x16_bf16 v[184:199], v[212:215], %[q3], %[m3]\n\t" \
    "v_exp_f32 v173, v173\n\t" \
    "v_exp_f32 v174, v174\n\t" \
    "v_exp_f32 v175, v175\n\t" \
    "v_exp_f32 v176, v176\n\t" \
    "v_exp_f32 v177, v177\n\t" \
    "v_smfmac_f32_32x32x32_bf16 %[o2], v[216:219], v[168:175], %[idx]\n\t" \
    "v_exp_f32 v178, v178\n\t" \
    "v_exp_f32 v179, v179\n\t" \
    "v_exp_f32 v180, v180\n\t" \
    "v_exp_f32 v181, v181\n\t" \
    "v_exp_f32 v182, v182\n\t" \
    "v_exp_f32 v183, v183\n\t" \
    "v_exp_f32 v184, v184\n\t" \
    "v_exp_f32 v185, v185\n\t" \
    "v_smfmac_f32_32x32x32_bf16 %[o2], v[220:223], v[176:183], %[idx]\n\t" \
    "v_exp_f32 v186, v186\n\t" \
    "v_exp_f32 v187, v187\n\t" \
    "s_waitcnt vmcnt(6)\n\t" \
    "v_exp_f32 v188, v188\n\t" \
    "v_mfma_f32_32x32x16_bf16 v[168:183], v[224:227], %[q0], %[m0]\n\t" \
    "v_exp_f32 v189, v189\n\t" \
    "v_exp_f32 v190, v190\n\t" \
    "v_exp_f32 v191, v191\n\t" \
    "v_exp_f32 v192, v192\n\t" \
    "v_exp_f32 v193, v193\n\t" \
    "v_smfmac_f32_32x32x32_bf16 %[o3], v[216:219], v[184:191], %[idx]\n\t" \
    "v_exp_f32 v194, v194\n\t" \
    "v_exp_f32 v195, v195\n\t" \
    "v_exp_f32 v196, v196\n\t" \
    "v_exp_f32 v197, v197\n\t" \
    "v_exp_f32 v198, v198\n\t" \
    "v_exp_f32 v199, v199\n\t" \
    "v_exp_f32 v168, v168\n\t" \
    "v_exp_f32 v169, v169\n\t" \
    "v_smfmac_f32_32x32x32_bf16 %[o3], v[220:223], v[192:199], %[idx]\n\t" \
    "buffer_load_dwordx4 v[212:215], %[gk], %[rs], %[ko] offen offset:2048 sc1\n\t" \
    "v_exp_f32 v170, v170\n\t" \
    "v_exp_f32 v171, v171\n\t" \
    "v_exp_f32 v172, v172\n\t" \
    "v_mfma_f32_32x32x16_bf16 v[184:199], v[224:227], %[q1], %[m1]\n\t" \
    "v_exp_f32 v173, v173\n\t" \
    "v_exp_f32 v174, v174\n\t" \
    "v_exp_f32 v175, v175\n\t" \
    "v_exp_f32 v176, v176\n\t" \
    "v_exp_f32 v177, v177\n\t" \
    "v_smfmac_f32_32x32x32_bf16 %[o0], v[228:231], v[168:175], %[idx]\n\t" \
    "s_mov_b64 exec, %[vm]\n\t" \
    "buffer_load_dwordx4 v[216:219], %[gv], %[rs], %[ko] offen offset:2048 sc1\n\t" \
    "buffer_load_dwordx4 v[220:223], %[gv], %[rs], %[ko] offen offset:2560 sc1\n\t" \
    "s_mov_b64 exec, -1\n\t" \
    "v_exp_f32 v178, v178\n\t" \
    "v_exp_f32 v179, v179\n\t" \
    "v_exp_f32 v180, v180\n\t" \
    "v_exp_f32 v181, v181\n\t" \
    "v_exp_f32 v182, v182\n\t" \
    "v_exp_f32 v183, v183\n\t" \
    "v_exp_f32 v184, v184\n\t" \
    "v_exp_f32 v185, v185\n\t" \
    "v_smfmac_f32_32x32x32_bf16 %[o0], v[232:235], v[176:183], %[idx]\n\t" \
    "v_exp_f32 v186, v186\n\t" \
    "v_exp_f32 v187, v187\n\t" \
    "v_exp_f32 v188, v188\n\t" \
    "v_mfma_f32_32x32x16_bf16 v[168:183], v[224:227], %[q2], %[m2]\n\t" \
    "v_exp_f32 v189, v189\n\t" \
    "v_exp_f32 v190, v190\n\t" \
    "v_exp_f32 v191, v191\n\t" \
    "v_exp_f32 v192, v192\n\t" \
    "v_exp_f32 v193, v193\n\t" \
    "v_smfmac_f32_32x32x32_bf16 %[o1], v[228:231], v[184:191], %[idx]\n\t" \
    "v_exp_f32 v194, v194\n\t" \
    "v_exp_f32 v195, v195\n\t" \
    "v_exp_f32 v196, v196\n\t" \
    "v_exp_f32 v197, v197\n\t" \
    "v_exp_f32 v198, v198\n\t" \
    "v_exp_f32 v199, v199\n\t" \
    "v_exp_f32 v168, v168\n\t" \
    "v_exp_f32 v169, v169\n\t" \
    "v_smfmac_f32_32x32x32_bf16 %[o1], v[232:235], v[192:199], %[idx]\n\t" \
    "v_exp_f32 v170, v170\n\t" \
    "v_exp_f32 v171, v171\n\t" \
    "v_exp_f32 v172, v172\n\t" \
    "v_mfma_f32_32x32x16_bf16 v[184:199], v[224:227], %[q3], %[m3]\n\t" \
    "v_exp_f32 v173, v173\n\t" \
    "v_exp_f32 v174, v174\n\t" \
    "v_exp_f32 v175, v175\n\t" \
    "v_exp_f32 v176, v176\n\t" \
    "v_exp_f32 v177, v177\n\t" \
    "v_smfmac_f32_32x32x32_bf16 %[o2], v[228:231], v[168:175], %[idx]\n\t" \
    "v_exp_f32 v178, v178\n\t" \
    "v_exp_f32 v179, v179\n\t" \
    "v_exp_f32 v180, v180\n\t" \
    "v_exp_f32 v181, v181\n\t" \
    "v_exp_f32 v182, v182\n\t" \
    "v_exp_f32 v183, v183\n\t" \
    "v_exp_f32 v184, v184\n\t" \
    "v_exp_f32 v185, v185\n\t" \
    "v_smfmac_f32_32x32x32_bf16 %[o2], v[232:235], v[176:183], %[idx]\n\t" \
    "v_exp_f32 v186, v186\n\t" \
    "v_exp_f32 v187, v187\n\t" \
    "s_waitcnt vmcnt(6)\n\t" \
    "v_exp_f32 v188, v188\n\t" \
    "v_mfma_f32_32x32x16_bf16 v[168:183], v[236:239], %[q0], %[m0]\n\t" \
    "v_exp_f32 v189, v189\n\t" \
    "v_exp_f32 v190, v190\n\t" \
    "v_exp_f32 v191, v191\n\t" \
    "v_exp_f32 v192, v192\n\t" \
    "v_exp_f32 v193, v193\n\t" \
    "v_smfmac_f32_32x32x32_bf16 %[o3], v[228:231], v[184:191], %[idx]\n\t" \
    "v_exp_f32 v194, v194\n\t" \
    "v_exp_f32 v195, v195\n\t" \
    "v_exp_f32 v196, v196\n\t" \
    "v_exp_f32 v197, v197\n\t" \
    "v_exp_f32 v198, v198\n\t" \
    "v_exp_f32 v199, v199\n\t" \
    "v_exp_f32 v168, v168\n\t" \
    "v_exp_f32 v169, v169\n\t" \
    "v_smfmac_f32_32x32x32_bf16 %[o3], v[232:235], v[192:199], %[idx]\n\t" \
    "buffer_load_dwordx4 v[224:227], %[gk], %[rs], %[ko] offen offset:3072 sc1\n\t" \
    "v_exp_f32 v170, v170\n\t" \
    "v_exp_f32 v171, v171\n\t" \
    "v_exp_f32 v172, v172\n\t" \
    "v_mfma_f32_32x32x16_bf16 v[184:199], v[236:239], %[q1], %[m1]\n\t" \
    "v_exp_f32 v173, v173\n\t" \
    "v_exp_f32 v174, v174\n\t" \
    "v_exp_f32 v175, v175\n\t" \
    "v_exp_f32 v176, v176\n\t" \
    "v_exp_f32 v177, v177\n\t" \
    "v_smfmac_f32_32x32x32_bf16 %[o0], v[240:243], v[168:175], %[idx]\n\t" \
    "s_mov_b64 exec, %[vm]\n\t" \
    "buffer_load_dwordx4 v[228:231], %[gv], %[rs], %[ko] offen offset:3072 sc1\n\t" \
    "buffer_load_dwordx4 v[232:235], %[gv], %[rs], %[ko] offen offset:3584 sc1\n\t" \
    "s_mov_b64 exec, -1\n\t" \
    "v_exp_f32 v178, v178\n\t" \
    "v_exp_f32 v179, v179\n\t" \
    "v_exp_f32 v180, v180\n\t" \
    "v_exp_f32 v181, v181\n\t" \
    "v_exp_f32 v182, v182\n\t" \
    "v_exp_f32 v183, v183\n\t" \
    "v_exp_f32 v184, v184\n\t" \
    "v_exp_f32 v185, v185\n\t" \
    "v_smfmac_f32_32x32x32_bf16 %[o0], v[244:247], v[176:183], %[idx]\n\t" \
    "s_add_u32 %[ko], %[ko], %[dl]\n\ts_xor_b32 %[dl], %[dl], %[dx]\n\t" \
    "v_exp_f32 v186, v186\n\t" \
    "v_exp_f32 v187, v187\n\t" \
    "v_exp_f32 v188, v188\n\t" \
    "v_mfma_f32_32x32x16_bf16 v[168:183], v[236:239], %[q2], %[m2]\n\t" \
    "v_exp_f32 v189, v189\n\t" \
    "v_exp_f32 v190, v190\n\t" \
    "v_exp_f32 v191, v191\n\t" \
    "v_exp_f32 v192, v192\n\t" \
    "v_exp_f32 v193, v193\n\t" \
    "v_smfmac_f32_32x32x32_bf16 %[o1], v[240:243], v[184:191], %[idx]\n\t" \
    "v_exp_f32 v194, v194\n\t" \
    "v_exp_f32 v195, v195\n\t" \
    "v_exp_f32 v196, v196\n\t" \
    "v_exp_f32 v197, v197\n\t" \
    "v_exp_f32 v198, v198\n\t" \
    "v_exp_f32 v199, v199\n\t" \
    "v_exp_f32 v168, v168\n\t" \
    "v_exp_f32 v169, v169\n\t" \
    "v_smfmac_f32_32x32x32_bf16 %[o1], v[244:247], v[192:199], %[idx]\n\t" \
    "s_sub_u32 %[n], %[n], 1\n\t" \
    "v_exp_f32 v170, v170\n\t" \
    "v_exp_f32 v171, v171\n\t" \
    "v_exp_f32 v172, v172\n\t" \
    "v_mfma_f32_32x32x16_bf16 v[184:199], v[236:239], %[q3], %[m3]\n\t" \
    "v_exp_f32 v173, v173\n\t" \
    "v_exp_f32 v174, v174\n\t" \
    "v_exp_f32 v175, v175\n\t" \
    "v_exp_f32 v176, v176\n\t" \
    "v_exp_f32 v177, v177\n\t" \
    "v_smfmac_f32_32x32x32_bf16 %[o2], v[240:243], v[168:175], %[idx]\n\t" \
    "v_exp_f32 v178, v178\n\t" \
    "v_exp_f32 v179, v179\n\t" \
    "v_exp_f32 v180, v180\n\t" \
    "v_exp_f32 v181, v181\n\t" \
    "v_exp_f32 v182, v182\n\t" \
    "v_exp_f32 v183, v183\n\t" \
    "v_exp_f32 v184, v184\n\t" \
    "v_exp_f32 v185, v185\n\t" \
    "v_smfmac_f32_32x32x32_bf16 %[o2], v[244:247], v[176:183], %[idx]\n\t" \
    "s_cmp_lg_u32 %[n], 0\n\t" \
    "v_exp_f32 v186, v186\n\t" \
    "v_exp_f32 v187, v187\n\t" \
    "s_waitcnt vmcnt(6)\n\t" \
    "v_exp_f32 v188, v188\n\t" \
    "v_mfma_f32_32x32x16_bf16 v[168:183], v[200:203], %[q0], %[m0]\n\t" \
    "v_exp_f32 v189, v189\n\t" \
    "v_exp_f32 v190, v190\n\t" \
    "v_exp_f32 v191, v191\n\t" \
    "v_exp_f32 v192, v192\n\t" \
    "v_exp_f32 v193, v193\n\t" \
    "v_smfmac_f32_32x32x32_bf16 %[o3], v[240:243], v[184:191], %[idx]\n\t" \
    "v_exp_f32 v194, v194\n\t" \
    "v_exp_f32 v195, v195\n\t" \
    "v_exp_f32 v196, v196\n\t" \
    "v_exp_f32 v197, v197\n\t" \
    "v_exp_f32 v198, v198\n\t" \
    "v_exp_f32 v199, v199\n\t" \
    "s_cbranch_scc1 1b\n\t" \
    "s_nop 1\n\t" \
    "v_smfmac_f32_32x32x32_bf16 %[o3], v[244:247], v[192:199], %[idx]\n\t" \
    "s_waitcnt vmcnt(0)\n\t" \
    "s_nop 15\n\t"
#define ATTN_LOOP_ASM_CLOB "v168", "v169", "v170", "v171", "v172", "v173", "v174", "v175", "v176", "v177", "v178", "v179", "v180", "v181", "v182", "v183", "v184", "v185", "v186", "v187", "v188", "v189", "v190", "v191", "v192", "v193", "v194", "v195", "v196", "v197", "v198", "v199", "v236", "v237", "v238", "v239", "v240", "v241", "v242", "v243", "v244", "v245", "v246", "v247"
#define ATTN_LOOP_ASM_KV(x) "+{v[200:203]}"(x[0]), "+{v[204:207]}"(x[1]), "+{v[208:211]}"(x[2]), "+{v[212:215]}"(x[3]), "+{v[216:219]}"(x[4]), "+{v[220:223]}"(x[5]), "+{v[224:227]}"(x[6]), "+{v[228:231]}"(x[7]), "+{v[232:235]}"(x[8])

__device__ __forceinline__ void glds16(const char* sbase, unsigned voff, unsigned lds_base) {
    unsigned sv;
    asm volatile("s_mov_b32 %0, m0\n\ts_mov_b32 m0, %3\n\ts_nop 0\n\tglobal_load_lds_dwordx4 %1, %2\n\ts_mov_b32 m0, %0"
                 : "=&s"(sv) : "v"(voff), "s"(sbase), "s"(lds_base) : "memory");
}
__device__ __forceinline__ unsigned pk2(float a, float b) { bf16x2 p; p[0] = (__bf16)a; p[1] = (__bf16)b; return __builtin_bit_cast(unsigned, p); }
__device__ __forceinline__ float bflo(unsigned u) { return __uint_as_float(u << 16); }
__device__ __forceinline__ float bfhi(unsigned u) { return __uint_as_float(u & 0xffff0000u); }
__device__ __forceinline__ float swap32_add(float v) {
    auto r = __builtin_amdgcn_permlane32_swap(__float_as_uint(v), __float_as_uint(v), false, false);
    return __uint_as_float(r[0]) + __uint_as_float(r[1]);
}
__device__ __forceinline__ float swap32_max(float v) {
    auto r = __builtin_amdgcn_permlane32_swap(__float_as_uint(v), __float_as_uint(v), false, false);
    return fmaxf(__uint_as_float(r[0]), __uint_as_float(r[1]));
}
template <int CTRL> __device__ __forceinline__ float dpp_max(float v) {
    return fmaxf(v, __int_as_float(__builtin_amdgcn_update_dpp(__float_as_int(v), __float_as_int(v), CTRL, 0xf, 0xf, false)));
}
__device__ __forceinline__ float sumsq4(unsigned a, unsigned b, unsigned c, unsigned d) {
    const bf16x2 x = __builtin_bit_cast(bf16x2, a), y = __builtin_bit_cast(bf16x2, b), z = __builtin_bit_cast(bf16x2, c), u = __builtin_bit_cast(bf16x2, d);
    return __builtin_amdgcn_fdot2_f32_bf16(x, x, __builtin_amdgcn_fdot2_f32_bf16(y, y, 0.f, false), false) +
           __builtin_amdgcn_fdot2_f32_bf16(z, z, __builtin_amdgcn_fdot2_f32_bf16(u, u, 0.f, false), false);
}
__device__ __forceinline__ u32x2 tr_read(lds_ptr p) { return __builtin_bit_cast(u32x2, __builtin_amdgcn_ds_read_tr16_b64_v4i16((LAS s16x4*)p)); }
__device__ __forceinline__ u32x4 wfrag(lds_ptr p, int second_off) {
    u32x2 a = tr_read(p), b = tr_read(p + second_off);
    return u32x4{a[0], a[1], b[0], b[1]};
}
struct WBase {
    lds_ptr lo, hi;
    __device__ __forceinline__ void set(lds_ptr p) { lo = p; unsigned t = (unsigned)(uintptr_t)p + 65536u; asm volatile("" : "+v"(t)); hi = (lds_ptr)(uintptr_t)t; }
    __device__ __forceinline__ lds_ptr at(int off) const { return off >= 65536 ? hi + (off - 65536) : lo + off; }
};
__device__ __forceinline__ u32x4 ld16(rsrc_t rs, unsigned voff, unsigned soff) { return __builtin_amdgcn_raw_buffer_load_b128(rs, voff, soff, 16); }
__device__ __forceinline__ u32x2 ld8(rsrc_t rs, unsigned voff, unsigned soff) { return __builtin_amdgcn_raw_buffer_load_b64(rs, voff, soff, 16); }
__device__ __forceinline__ void st16(u32x4 v, rsrc_t rs, unsigned voff, unsigned soff) { __builtin_amdgcn_raw_buffer_store_b128(v, rs, voff, soff, 16); }
__device__ __forceinline__ void st4(unsigned v, rsrc_t rs, unsigned voff, unsigned soff) { __builtin_amdgcn_raw_buffer_store_b32(v, rs, voff, soff, 16); }

__device__ __forceinline__ GAS u64* mbox_(char* ws, int edge, int consumer, int slot, unsigned sides) { return (GAS u64*)(ws + arena(consumer & 7, sides) + A_FLAG + stripe((unsigned)((edge * 32 + (consumer >> 3)) * 64 + slot) * 8u)); }
#define mbox(ws, e, c, s) mbox_(ws, e, c, s, sides)
__device__ __forceinline__ GAS u64* mbox0(char* ws, int consumer, int slot) { return (GAS u64*)(ws + WS_FLAG0 + (unsigned)(consumer * 64 + slot) * 8u); }
__device__ __forceinline__ void post(GAS u64* p, u64 v, unsigned payload = 0) {
    asm volatile("" : "+s"(v));
    __hip_atomic_store(p, v | payload, __ATOMIC_RELAXED, __HIP_MEMORY_SCOPE_AGENT);
}
template <int DELAY, int SLEEP>
__device__ __forceinline__ u64 wait_tags(GAS u64* p, u64 tag) {
    u64 v = 0;
    if (DELAY) __builtin_amdgcn_s_sleep(DELAY);
#pragma unroll 1
    for (unsigned spins = 0; spins < SPIN_CAP; ++spins) {
        v = __hip_atomic_load(p, __ATOMIC_RELAXED, __HIP_MEMORY_SCOPE_AGENT);
        if (__all(((v ^ tag) >> 16) == 0)) break;
        __builtin_amdgcn_s_sleep(SLEEP);
    }
    asm volatile("" ::: "memory");
    return v;
}

__device__ __forceinline__ void emit_x(rsrc_t rs, unsigned xo, unsigned so, int lane, const unsigned (&pk)[8]) {
    float s = 0.f;
    st16(u32x4{pk[0], pk[1], pk[2], pk[3]}, rs, lane * 16, xo);
    st16(u32x4{pk[4], pk[5], pk[6], pk[7]}, rs, lane * 16 + 1024, xo);
#pragma unroll
    for (int j = 0; j < 8; j += 2) {
        const bf16x2 one = __builtin_bit_cast(bf16x2, 0x3f803f80u);
        s += __builtin_amdgcn_fdot2_f32_bf16(__builtin_bit_cast(bf16x2, pk[j]), one, __builtin_amdgcn_fdot2_f32_bf16(__builtin_bit_cast(bf16x2, pk[j + 1]), one, 0.f, false), false);
    }
    const float sq = sumsq4(pk[0], pk[1], pk[2], pk[3]) + sumsq4(pk[4], pk[5], pk[6], pk[7]);
    auto r = __builtin_amdgcn_permlane32_swap(__float_as_uint(s), __float_as_uint(sq), false, false);
    st4(__float_as_uint(__uint_as_float(r[0]) + __uint_as_float(r[1])), rs, (lane & 31) * 8 + (lane >> 5) * 4, so);
}

template <int NA, int NSTEP, int PRE, unsigned long long BARS, int PF, int SECOND, bool MORE, class WOFF, class REFILL, class FILL>
__device__ __forceinline__ void gemm_loop(f32x16* acc, u32x4 (&xf)[PF], u32x4 (&wq)[PRE + 1][NA], const WBase (&wb)[NA], WOFF woff, REFILL refill, FILL fill) {
#pragma unroll
    for (int s = 0; s < NSTEP; ++s) {
#pragma unroll
        for (int a = 0; a < NA; ++a) {
            if (s + 2 >= PRE && (MORE || s + 2 < NSTEP)) wq[(s + 2) % (PRE + 1)][a] = wfrag(wb[a].at(woff(s + 2)), SECOND);
            if (s == 0) MFMA_ASM0(acc[a], wq[0][a], xf[0]); else MFMA_ASM(acc[a], wq[s % (PRE + 1)][a], xf[s % PF]);
            fill(s, a);
        }
        refill(s);
        if ((BARS >> s) & 1) BAR_RAW();
    }
    if (MORE) {
#pragma unroll
        for (int a = 0; a < NA; ++a) { const u32x4 t0 = wq[NSTEP % (PRE + 1)][a], t1 = wq[(NSTEP + 1) % (PRE + 1)][a]; wq[0][a] = t0; wq[1][a] = t1; }
    }
}
__device__ __forceinline__ u32x4 feat_frag(float cs, unsigned b) {
    const unsigned hi = pk2(cs, cs) & 0xffffu, lo = pk2(cs - bflo(hi), 0.f);
    return u32x4{hi | (lo << 16), hi | (b << 16), b, 0u};
}
__device__ __forceinline__ u32x4 tok_frag(float mean, float isc, int h) {
    const unsigned mh = pk2(-mean, -mean), ih = pk2(isc, isc) & 0xffffu;
    const unsigned ml = pk2(-mean - bflo(mh), 0.f), il = pk2(isc - bflo(ih), 0.f);
    return h ? u32x4{0u, 0u, 0u, 0u} : u32x4{mh, ml | (ih << 16), il, 0u};
}
__device__ __forceinline__ void row_stats(const u32x2 (&sp)[8], float& mean, float& var) {
    float sm = ((__uint_as_float(sp[0][0]) + __uint_as_float(sp[1][0])) + (__uint_as_float(sp[2][0]) + __uint_as_float(sp[3][0]))) +
               ((__uint_as_float(sp[4][0]) + __uint_as_float(sp[5][0])) + (__uint_as_float(sp[6][0]) + __uint_as_float(sp[7][0])));
    float sq = ((__uint_as_float(sp[0][1]) + __uint_as_float(sp[1][1])) + (__uint_as_float(sp[2][1]) + __uint_as_float(sp[3][1]))) +
               ((__uint_as_float(sp[4][1]) + __uint_as_float(sp[5][1])) + (__uint_as_float(sp[6][1]) + __uint_as_float(sp[7][1])));
    sm = swap32_add(sm); sq = swap32_add(sq);
    mean = sm * (1.0f / DM);
    var = fmaxf(sq * (1.0f / DM) - mean * mean, 0.f);
}

__global__ __launch_bounds__(512, 2) void k_all(const u16* __restrict__ x0, const u16* __restrict__ wqkv, const u16* __restrict__ wo, const u16* __restrict__ w1,
                                                const u16* __restrict__ b1, const u16* __restrict__ w2, const u16* __restrict__ b2, u16* __restrict__ out, char* ws) {
    extern __shared__ __attribute__((aligned(16))) char smem[];
    const int w = __builtin_amdgcn_readfirstlane(threadIdx.x >> 6);
    const lds_ptr L = (lds_ptr)smem;
    const unsigned lds0 = (unsigned)(uintptr_t)smem;
    const rsrc_t rs = __builtin_amdgcn_make_buffer_rsrc(ws, 0, WS_END, 0x00020000);
    const bool comp = w < 4;
    unsigned sides = 0;
#define COORDS() \
    int lane; asm volatile("v_mbcnt_lo_u32_b32 %0, -1, 0\n\tv_mbcnt_hi_u32_b32 %0, -1, %0" : "=v"(lane)); int b = blockIdx.x; OPAQUE_S(b); int lw = w & 3; OPAQUE_S(lw); \
    const int c = lane & 31, h = lane >> 5, q4 = (lane & 15) >> 2, g1 = (lane >> 4) & 1, l16 = lane & 15; \
    const int xg = b & 7, i32 = b >> 3; \
    const int rbQ = (xg >> 1) * 4 + (i32 >> 3), cbQ = (xg & 1) * 8 + (i32 & 7), TQK = rbQ * 4 + lw;     \
    const int rbA = xg * 2 + (i32 >> 4), cbA = i32 & 15;     \
    const int cbU = (xg & 1) * 7 + ((i32 & 7) < 7 ? (i32 & 7) : 6), TA = TQK;     \
    const bool actU = (i32 & 7) < 7; (void)actU; \
    const int rbB = xg * 4 + (i32 >> 3), cbB = i32 & 7;                                                 \
    const int TB = rbB * 2 + (lw & 1), kh = lw >> 1, stripB = cbB * 2 + kh; \
    const int trq = 4 * h + q4, trc = g1 * 32 + (lane & 3) * 8;                          \
    const int grp = (((rbQ & 1) * 16 + l16) << 3) | (rbQ >> 1);                                             \
    const int xcons = (((rbA & 3) * 8 + (l16 & 7)) << 3) | ((rbA >> 2) * 2 + (l16 >> 3));     \
    (void)rbQ; (void)cbQ; (void)TQK; (void)xcons; (void)grp; (void)rbA; (void)c; (void)q4; (void)g1; (void)xg; (void)cbU; (void)TA; (void)TB; (void)stripB; (void)trq; (void)trc; (void)cbA; (void)cbB; (void)kh
    auto touch = [&](const GAS u64* p) {
        unsigned sv;
        asm volatile("s_mov_b32 %0, m0\n\ts_mov_b32 m0, %2\n\ts_nop 0\n\tglobal_load_lds_dword %1, off sc1\n\ts_mov_b32 m0, %0" : "=&s"(sv) : "v"(p), "s"(lds0 + L_EX + 20480) : "memory");
    };
    auto early = [&]() {
        {
            COORDS();
            u64 pc; asm volatile("s_getpc_b64 %0" : "=s"(pc));
            const int piece = i32 * 4 + lw;
            if (piece < CODE_KB) glds16((const char*)((pc & ~255ull) + piece * 1024), lane * 16, lds0 + L_EX + 12288 + lw * 1024);
        }
        if (w == 7) {
            COORDS();
            const GAS char* p = (const GAS char*)mbox0(ws, ((lane >> 2) & 1) * 256 + b, (lane & 3) * 16);
            {
                const int pc = b * 4;
                if (lane == 8) p = (const GAS char*)(pc < 128 ? (const char*)wo + pc * 4096 : pc < 576 ? (const char*)w1 + (pc - 128) * 4096 : (const char*)w2 + (pc - 576) * 4096);
            }
            touch((const GAS u64*)p);
        }
        if ((w == 4 || w == 5) && blockIdx.x < 16) {
            COORDS();
            st4(0u, rs, h * 8192 + c * 4, WS_ARENA + xg * ARENA + A_PROBE + lw * 256 + i32 * 512);
        }
    };
    asm volatile("" :: "s"(x0), "s"(wqkv), "s"(wo), "s"(w1), "s"(b1), "s"(w2), "s"(b2), "s"(out), "s"(ws));
    if (comp) {
        COORDS();
        const char* src = (const char*)x0 + (size_t)TB * 32768 + stripB * 64;
        const unsigned vo = (lane >> 2) * 1024 + (lane & 3) * 16;
        glds16(src, vo, lds0 + L_EX + w * 2048); glds16(src + 16384, vo, lds0 + L_EX + w * 2048 + 1024);
    } else {
        COORDS();
        unsigned vo[3];
#pragma unroll
        for (int i = 0; i < 3; ++i) { const int e = i * 64 + lane, k = (e * 683) >> 13, cc = e - k * 12; vo[i] = k * 3072 + cc * 16; }
#pragma unroll 1
        for (int j = 0; j < 8; ++j) {
            const char* src = (const char*)wqkv + (size_t)((j >> 1) * 128 + (j & 1) * 16 + lw * 32) * 3072 + cbQ * 192;
            const unsigned dst = lds0 + (((j >> 1) + 1) & 3) * 32768 + lw * 6144 + (j & 1) * 3072;
#pragma unroll
            for (int i = 0; i < 3; ++i) glds16(src, vo[i], dst + i * 1024);
            if (j == 3) { early(); BAR_RAW(); }
        }
    }
    auto issue_down = [&](const u16* W, int KS, int ch, int slot) {
        COORDS();
        const int r8 = lane >> 3;
        const char* src = (const char*)W + (size_t)((lw >> 1) * KS * 16 + (KS == 56 ? (ch * 2 + (lw & 1)) * 128 : ch * 128 + (lw & 1) * 64)) * 1024 + cbB * 128;
        const unsigned vo = r8 * 1024 + (((lane & 7) ^ (((r8 >> 1) & 1) << 2)) * 16);
#pragma unroll
        for (int i = 0; i < 8; ++i) glds16(src + i * 8192, vo, lds0 + slot * 32768 + lw * 8192 + i * 1024);
    };
    auto issue_up = [&](int ch) {
        COORDS();
        const int r8 = lane >> 3;
        const char* src = (const char*)w1 + (size_t)((ch & 1) * 256 + lw * 64) * (FF * 2) + cbU * 256 + (ch >> 1) * 128;
        const unsigned vo = r8 * (FF * 2) + (((lane & 7) ^ (((r8 >> 1) & 1) << 2)) * 16);
#pragma unroll
        for (int i = 0; i < 8; ++i) glds16(src + i * 8 * (FF * 2), vo, lds0 + ((ch + 2) & 3) * 32768 + lw * 8192 + i * 1024);
    };
    auto fetch_qkv = [&](u32x4 (&r)[8], int ch) {
        COORDS();
        const rsrc_t rw = __builtin_amdgcn_make_buffer_rsrc((void*)wqkv, 0, 512 * 3072, 0x00020000);
#pragma unroll
        for (int i = 0; i < 3; ++i) {
            const int e = i * 64 + lane, k = (e * 683) >> 13, cc = e - k * 12;
            const unsigned vo = k * 3072 + cc * 16, so = (ch * 128 + lw * 32) * 3072 + cbQ * 192;
            r[i] = __builtin_amdgcn_raw_buffer_load_b128(rw, vo, so, 0);
            r[3 + i] = __builtin_amdgcn_raw_buffer_load_b128(rw, vo, so + 16 * 3072, 0);
        }
    };
    auto fetch_w2 = [&](u32x4 (&r)[8], int ch) {
        COORDS();
        const rsrc_t rw = __builtin_amdgcn_make_buffer_rsrc((void*)w2, 0, FF * 1024, 0x00020000);
        const int r8 = lane >> 3;
        const unsigned vo = r8 * 1024 + (((lane & 7) ^ (((r8 >> 1) & 1) << 2)) * 16), gi = ch * 2 + (lw & 1);
        const unsigned so = ((lw >> 1) * 896 + (gi >= 7 ? (gi - 7) * 128 + 64 : gi * 128)) * 1024 + cbB * 128;
#pragma unroll
        for (int i = 0; i < 8; ++i) r[i] = __builtin_amdgcn_raw_buffer_load_b128(rw, vo, so + i * 8192, 0);
    };
    auto fetch_up = [&](u32x4 (&r)[8], int ch) {
        COORDS();
        const rsrc_t rw = __builtin_amdgcn_make_buffer_rsrc((void*)w1, 0, 512 * FF * 2, 0x00020000);
        const int r8 = lane >> 3;
        const unsigned vo = r8 * (FF * 2) + (((lane & 7) ^ (((r8 >> 1) & 1) << 2)) * 16), so = ((ch & 1) * 256 + lw * 64) * (FF * 2) + cbU * 256 + (ch >> 1) * 128;
#pragma unroll
        for (int i = 0; i < 8; ++i) r[i] = __builtin_amdgcn_raw_buffer_load_b128(rw, vo, so + i * 8 * (FF * 2), 0);
    };
    auto put = [&](const u32x4 (&r)[8], int slot, int n, bool slow = false) {
        COORDS();
        const lds_ptr p = L + slot * 32768 + lw * (n * 1024) + lane * 16;
#pragma unroll
        for (int i = 0; i < 8; ++i)
            if (i < n) {
                *(LAS u32x4*)(p + i * 1024) = r[i];
                if (slow) { asm volatile("" ::: "memory"); __builtin_amdgcn_s_sleep(1); }
            }
    };

    const u64 ep = __hip_atomic_load((GAS u64*)(ws + WS_EPOCH), __ATOMIC_RELAXED, __HIP_MEMORY_SCOPE_AGENT);
    u64 epn, tag;
#define TAG_SET() epn = ((u64)(unsigned)__builtin_amdgcn_readfirstlane((unsigned)ep) | ((u64)(unsigned)__builtin_amdgcn_readfirstlane((unsigned)(ep >> 32)) << 32)) + 1; tag = epn << 16
    if (comp) {
        COORDS();
        unsigned xr[8];
        const lds_ptr tb = L + L_EX + w * 2048;
        WAITVM(1);
#pragma unroll
        for (int q = 0; q < 4; ++q) { u32x2 t = *(LAS u32x2*)(tb + c * 64 + q * 16 + h * 8); xr[2 * q] = t[0]; xr[2 * q + 1] = t[1]; }
        emit_x(rs, WS_X0 + TB * 32768 + stripB * 2048, WS_ST0 + TB * 4096 + stripB * 256, lane, xr);
        if (w == 0 && lane == 0) *(LAS unsigned*)(L + T_KN + 384) = 0;
        BAR();
        TAG_SET();
        WAITVM(0);
        unsigned old = 0;
        if (lane == 0) old = __hip_atomic_fetch_add((LAS unsigned*)(L + T_KN + 384), 1u, __ATOMIC_RELAXED, __HIP_MEMORY_SCOPE_WORKGROUP);
        if (__builtin_amdgcn_readfirstlane(old) == 3 && lane < 16) post(mbox0(ws, xcons, cbA), tag);
    } else {
        COORDS();
        {
            f32x16 cs[3];
            unsigned one2 = 0x3f803f80u; PIN(one2);
            const u32x4 ones = {one2, one2, one2, one2};
#pragma unroll
            for (int a = 0; a < 3; ++a) cs[a] = f32x16{};
#pragma unroll
            for (int ch = 0; ch < 4; ++ch) {
                if (ch == 0) WAITVM(18); else if (ch == 1) WAITVM(12); else if (ch == 2) WAITVM(6); else WAITVM(0);
#pragma unroll
                for (int s = 0; s < 2; ++s)
#pragma unroll
                    for (int a = 0; a < 3; ++a)
                        cs[a] = MFMA32(__builtin_bit_cast(bf16x8, wfrag(L + ((ch + 1) & 3) * 32768 + (lw * 32 + s * 16 + trq) * 192 + trc + a * 64, 1536)), __builtin_bit_cast(bf16x8, ones), cs[a]);
            }
            if (c == 0) {
#pragma unroll
                for (int a = 0; a < 3; ++a)
#pragma unroll
                    for (int q = 0; q < 4; ++q) *(LAS f32x4*)(L + T_PART + lw * 512 + a * 128 + q * 32 + h * 16) = f32x4{cs[a][4 * q], cs[a][4 * q + 1], cs[a][4 * q + 2], cs[a][4 * q + 3]};
            }
        }
        TAG_SET();
        BAR();
        if (lane < 32 && lw < 3) {
            const lds_ptr p = L + T_PART + lw * 128 + lane * 4;
            *(LAS u32x4*)(L + T_FQ + lw * 512 + lane * 16) = feat_frag((*(LAS float*)p + *(LAS float*)(p + 512)) + (*(LAS float*)(p + 1024) + *(LAS float*)(p + 1536)), 0u);
        }
        BAR();
        auto wwarm = [&]() {
            COORDS();
            const int pc = b * 4 + lw;
            const char* src = pc < 128 ? (const char*)wo + pc * 4096 : pc < 576 ? (const char*)w1 + (pc - 128) * 4096 : (const char*)w2 + (pc - 576) * 4096;
#pragma unroll
            for (int i = 0; i < 4; ++i) glds16(src + i * 1024, lane * 16, lds0 + lw * 8192 + i * 1024);
        };
        auto fetch_next = [&](u32x4 (&r)[8], int ch, bool on) {
            if (on) fetch_qkv(r, ch);
            else {
#pragma unroll
                for (int i = 0; i < 6; ++i) asm volatile("" : "=v"(r[i]));
            }
        };
#pragma unroll 1
        for (int layer = 0; layer < 2; ++layer) {
            OPAQUE_S(layer);
            const int e0 = layer * 5;
            DRAIN_BAR();
            BAR();
            if (layer == 0) __builtin_amdgcn_s_setprio(2);
            {
                COORDS();
                {
                    __builtin_amdgcn_s_sleep(10);
                    u32x4 t[4];
#pragma unroll
                    for (int i = 0; i < 4; ++i)
                        t[i] = ld16(rs, lane * 16 + (i >> 1) * (A_V - A_K), qkv_off(layer, xg * 4 + (i32 >> 3), sides) + A_K + (lw + (i & 1) * 4) * (8192 << layer) + (i32 & 7) * 1024);
#pragma unroll
                    for (int i = 0; i < 4; ++i) asm volatile("" :: "v"(t[i]));
                }
                if (b < 16 && w == 4 && layer == 0) {
                    unsigned t[4];
#pragma unroll
                    for (int i = 0; i < 4; ++i) {
                        const char* p = ws + WS_ARENA + xg * ARENA + A_PROBE + (i & 1) * 8192 + (i >> 1) * 256 + i32 * 512;
                        u64 t0, t1; unsigned v;
                        asm volatile("s_memtime %0\n\ts_waitcnt lgkmcnt(0)\n\tglobal_load_dword %2, %3, %4 sc1\n\ts_waitcnt vmcnt(0)\n\ts_memtime %1\n\ts_waitcnt lgkmcnt(0)"
                                     : "=&s"(t0), "=&s"(t1), "=&v"(v) : "v"(0), "s"(p) : "memory");
                        t[i] = (unsigned)(t1 - t0);
                    }
                    const unsigned near1 = min(t[1], t[3]) < min(t[0], t[2]), half = (__builtin_amdgcn_s_getreg((3 << 11) | 20) >> 2) & 1;
                    if (lane < 8) post(mbox0(ws, lane, 32 + b), tag, near1 | (half << 1));
                }
                __builtin_amdgcn_s_setprio(0);
                if (layer == 0) { __builtin_amdgcn_s_sleep(20); wwarm(); WAITVM(0); __builtin_amdgcn_s_sleep(1); }
                PACE(); issue_down(wo, 16, 0, 0); PACE(); issue_down(wo, 16, 1, 1); PACE(); issue_up(0); PACE(); issue_up(1);
            }
            COORDS();
            f32x16 cs[2]; cs[0] = f32x16{}; cs[1] = f32x16{};
            unsigned one2 = 0x3f803f80u; PIN(one2);
            const u32x4 ones = {one2, one2, one2, one2};
            auto csum = [&](int ch) {
                if (layer == 0) {
                    if ((ch & 1) == 0) { cs[0] = f32x16{}; cs[1] = f32x16{}; }
#pragma unroll
                    for (int s = 0; s < 4; ++s)
#pragma unroll
                        for (int a = 0; a < 2; ++a)
                            cs[a] = MFMA32(__builtin_bit_cast(bf16x8, wfrag(L + ((ch + 2) & 3) * 32768 + (lw * 64 + s * 16 + trq) * 128 + trc + ((a ^ (q4 >> 1)) * 64), 1024)), __builtin_bit_cast(bf16x8, ones), cs[a]);
                    if ((ch & 1) && c == 0) {
#pragma unroll
                        for (int a = 0; a < 2; ++a)
#pragma unroll
                            for (int q = 0; q < 4; ++q) *(LAS f32x4*)(L + T_PART + lw * 512 + a * 128 + q * 32 + h * 16) = f32x4{cs[a][4 * q], cs[a][4 * q + 1], cs[a][4 * q + 2], cs[a][4 * q + 3]};
                    }
                }
            };
            unsigned bias[2] = {0u, 0u};
            if (layer == 0) { bias[0] = b1[cbU * 128 + lane]; bias[1] = b1[cbU * 128 + 64 + lane]; }
            auto cred = [&](int pair) {
                if (layer == 0 && lw == 0) {
                    const lds_ptr p = L + T_PART + lane * 4;
                    *(LAS u32x4*)(L + T_FU + pair * 1024 + lane * 16) = feat_frag((*(LAS float*)p + *(LAS float*)(p + 512)) + (*(LAS float*)(p + 1024) + *(LAS float*)(p + 1536)), bias[pair]);
                }
            };
            u32x4 R0[8], R1[8], R2[8], R3[8], R4[8], R5[8], R6[8];
            WAITVM(0);
            __builtin_amdgcn_s_setprio(2);
            csum(0); csum(1);
            if (w == 7 && layer == 0) {
                const u64 v = wait_tags<0, 8>(mbox0(ws, xg, 32 + l16), tag);
                const unsigned near1 = (unsigned)__ballot(v & 1), half = (unsigned)__ballot(v & 2);
                const unsigned sd = (__builtin_popcount(near1 ^ half) > 16 ? ~half : half) & 0xffu;
                if (lane == 0) *(LAS unsigned*)(L + T_KN + 320) = sd;
                const GAS u64* tp = mbox_(ws, 2 + (c >> 2), b, (c & 3) * 16, sd);
                touch(tp);
            }
            __builtin_amdgcn_s_setprio(0);
            PACE(); fetch_up(R0, 2); PACE(); fetch_up(R1, 3); PACE(); fetch_w2(R2, 2); PACE(); fetch_w2(R3, 3); PACE(); fetch_w2(R4, 4); PACE(); fetch_w2(R5, 5); PACE(); fetch_w2(R6, 6);
            BAR(); cred(0);
            sides = __builtin_amdgcn_readfirstlane(*(LAS unsigned*)(L + T_KN + 320));
            BAR();
            BAR(); put(R0, 0, 8); put(R1, 1, 8);
            BAR();
            BAR();
            csum(2); csum(3);
            BAR(); cred(1);
            __builtin_amdgcn_s_sleep(36); fetch_next(R0, 0, layer == 0); fetch_next(R1, 1, layer == 0);
            BAR(); issue_down(w2, 56, 0, 2); issue_down(w2, 56, 1, 3);
            WAITVM(0);
            __builtin_amdgcn_s_setprio(2);
            BAR();
            if (w == 5 && actU && lane < 16) post(mbox(ws, e0 + 4, grp, cbU), tag);
            put(R2, 0, 8); put(R3, 1, 8);
            if (w == 4) wait_tags<0, 2>(mbox(ws, e0 + 4, b, l16 < 14 ? l16 : 0), tag);
            {
                GAS u64* p = mbox(ws, e0 + 4, grp, 16 + cbU); PIN(p);
                BAR();
                if (w == 5 && lane < 16 && actU) post(p, tag);
            }
            put(R4, 2, 8); put(R5, 3, 8);
            if (w == 4) wait_tags<0, 2>(mbox(ws, e0 + 4, b, 16 + (l16 < 14 ? l16 : 0)), tag);
            __builtin_amdgcn_s_setprio(0);
            BAR(); fetch_next(R2, 2, layer == 0);
            BAR(); put(R6, 0, 8, true);
            BAR(); fetch_next(R3, 3, layer == 0);
            BAR();
            if (layer == 0) { put(R0, 1, 6); put(R1, 2, 6); BAR(); put(R2, 3, 6); put(R3, 0, 6); BAR(); BAR(); }
        }
        return;
    }

    for (int layer = 0; layer < 2; ++layer) {
        OPAQUE_S(layer);
        const int e0 = layer * 5;
        {
            COORDS();
            if (w == 0) wait_tags<8, 2>(layer == 0 ? mbox0(ws, b, l16) : mbox(ws, 5, b, l16), tag);
            BAR_RAW();
            constexpr int PF = 12;
            const unsigned xo = layer == 0 ? WS_X0 + TQK * 32768 : x_off(2, TQK, sides), xs = layer == 0 ? 8192 : 16384;
            const unsigned so = layer == 0 ? WS_ST0 + TQK * 4096 : st_off(2, TQK, sides);
            u32x4 xf[PF];
#pragma unroll
            for (int s = 0; s < PF; ++s) xf[s] = ld16(rs, lane * 16 + (s & 3) * 1024, xo + (s >> 3) * xs + ((s >> 2) & 1) * 4096);
            WBase wb[3]; u32x4 w0[3][3];
            wb[0].set(L + trq * 192 + trc);
#pragma unroll
            for (int a = 0; a < 3; ++a) { wb[a].lo = wb[0].lo + a * 64; wb[a].hi = wb[0].hi + a * 64; w0[0][a] = wfrag(wb[a].at(32768), 1536); w0[1][a] = wfrag(wb[a].at(32768 + 3072), 1536); }
            u32x2 sp[8];
            f32x16 acc[3];
            u32x4 ff[3], tf;
            float rstd;
            gemm_loop<3, 32, 2, (1ull << 12), PF, 1536, false>(acc, xf, w0, wb, [](int s) { return (((s >> 3) + 1) & 3) * 32768 + (s & 7) * 3072; },
                [&](int s) {
                    if (s + PF < 32) xf[s % PF] = ld16(rs, lane * 16 + ((s + PF) & 3) * 1024, xo + ((s + PF) >> 3) * xs + (((s + PF) >> 2) & 1) * 4096);
                    if (s >= 20 && s < 28) sp[s - 20] = ld8(rs, h * 2048 + c * 8 + (s - 20) * 256, so);
                    if (s == 29) {
                        float mean, var; row_stats(sp, mean, var);
                        rstd = __builtin_amdgcn_rsqf(var + LN_EPS); tf = tok_frag(mean, 0.f, h);
                        PIN(rstd); PIN(tf);
                    }
                    if (s == 30) {
#pragma unroll
                        for (int a = 0; a < 3; ++a) ff[a] = *(LAS u32x4*)(L + T_FQ + a * 512 + c * 16);
                    }
                }, [](int, int) {});
#pragma unroll
            for (int a = 0; a < 3; ++a) MFMA_ASM(acc[a], ff[a], tf);
            MFMA_DONE3(acc[0], acc[1], acc[2]);
            const lds_ptr tb = L + L_EX + w * 6144;
            unsigned pk[3][8];
#pragma unroll
            for (int a = 0; a < 3; ++a) { acc[a] = acc[a] * ((cbQ * 3 + a) < 16 ? rstd * QSCALE : rstd); PIN(acc[a]); }
#pragma unroll
            for (int a = 0; a < 3; ++a)
#pragma unroll
                for (int j = 0; j < 8; ++j) pk[a][j] = pk2(acc[a][2 * j], acc[a][2 * j + 1]);
#pragma unroll
            for (int a = 0; a < 3; ++a) {
                const int g = cbQ * 3 + a;
                if (g < 32) {
                    const unsigned so = qkv_off(layer, (g & 15) * 2, sides) + (g < 16 ? A_Q : A_K) + stripe_p(TQK * 1024, xs);
                    st16(u32x4{pk[a][0], pk[a][1], pk[a][2], pk[a][3]}, rs, lane * 16, so);
                    st16(u32x4{pk[a][4], pk[a][5], pk[a][6], pk[a][7]}, rs, lane * 16, so + 131072);
                } else {
#pragma unroll
                    for (int q = 0; q < 4; ++q) *(LAS u32x2*)(tb + a * 2048 + c * 64 + ((q ^ (c >> 1)) & 3) * 16 + h * 8) = u32x2{pk[a][2 * q], pk[a][2 * q + 1]};
                }
            }
            if (cbQ >= 10) {
#pragma unroll
                for (int a = 0; a < 3; ++a) {
                    const int g = cbQ * 3 + a;
                    if (g >= 32) {
#pragma unroll
                        for (int s = 0; s < 2; ++s) {
                            const int r0 = s * 16 + 8 * h + q4, r1 = r0 + 4, cq = g1 * 2 + ((lane & 3) >> 1);
                            const u32x2 lo = tr_read(tb + a * 2048 + r0 * 64 + ((cq ^ (r0 >> 1)) & 3) * 16 + (lane & 1) * 8);
                            const u32x2 hi = tr_read(tb + a * 2048 + r1 * 64 + ((cq ^ (r1 >> 1)) & 3) * 16 + (lane & 1) * 8);
                            st16(u32x4{lo[0], lo[1], hi[0], hi[1]}, rs, g1 * 131072 + h * 256 + l16 * 16 + s * 512, qkv_off(layer, (g & 15) * 2, sides) + A_V + stripe_p(TQK * 1024, xs));
                        }
                    }
                }
            }
            if (cbQ <= 10) {
                float m[3];
#pragma unroll
                for (int a = 0; a < 3; ++a) {
                    const float n0 = sumsq4(pk[a][0], pk[a][1], pk[a][2], pk[a][3]), n1 = sumsq4(pk[a][4], pk[a][5], pk[a][6], pk[a][7]);
                    auto r = __builtin_amdgcn_permlane32_swap(__float_as_uint(n0), __float_as_uint(n1), false, false);
                    m[a] = __uint_as_float(r[0]) + __uint_as_float(r[1]);
                }
#pragma unroll
                for (int a = 0; a < 3; ++a) m[a] = dpp_max<0x111>(m[a]);
#pragma unroll
                for (int a = 0; a < 3; ++a) m[a] = dpp_max<0x112>(m[a]);
#pragma unroll
                for (int a = 0; a < 3; ++a) m[a] = dpp_max<0x114>(m[a]);
#pragma unroll
                for (int a = 0; a < 3; ++a) m[a] = dpp_max<0x118>(m[a]);
#pragma unroll
                for (int a = 0; a < 3; ++a) m[a] = dpp_max<0x142>(m[a]);
                if (c == 31) {
#pragma unroll
                    for (int a = 0; a < 3; ++a) *(LAS float*)(L + T_KN + (a * 2 + h) * 16 + w * 4) = m[a];
                }
            }
        }
        {
            COORDS();
            const int head = xg * 4 + (i32 >> 3), qb = i32 & 7;
            const int g = cbQ * 3 + (lane >> 4), type = g >> 4, ch = (g & 15) * 2 + ((lane >> 3) & 1), cq = lane & 7;
            const int mc = w == 1 ? (((ch & 3) * 8 + cq) << 3) | (ch >> 2) : b;
            const int ms = w == 1 ? (type == 0 ? 32 + (rbQ & 1) : (type - 1) * 16 + rbQ) : (lane < 32 ? lane : 32 + (lane & 1));
            GAS u64* mp = layer == 0 ? mbox0(ws, 256 + mc, ms) : mbox(ws, 6, mc, ms);
            const unsigned xs = 8192 << layer;
            PIN(mp);
            DRAIN_BAR();
            if (w == 1) {
                const f32x4 m4 = *(LAS f32x4*)(L + T_KN + (lane >> 3) * 16);
                const unsigned m = (__float_as_uint(fmaxf(fmaxf(m4[0], m4[1]), fmaxf(m4[2], m4[3]))) + 0xffffu) >> 16;
                if (lane < 48 && (type != 0 || cq == (rbQ >> 1))) post(mp, tag, m & 0xffffu);
            } else if (w == 0) {
                float kn = bflo((unsigned)wait_tags<0, 1>(mp, tag));
                kn = dpp_max<0x111>(kn); kn = dpp_max<0x112>(kn); kn = dpp_max<0x114>(kn); kn = dpp_max<0x118>(kn);
                if (lane == 15 || lane == 47) *(LAS float*)(L + T_KN + 128 + h * 4) = kn;
            }
            BAR();
            const int TQ = qb * 8 + (lw & 1) * 4;
            const unsigned hb = qkv_off(layer, head, sides);
            unsigned ko = hb + A_K + kh * 4 * xs;
            u32x4 q[4];
#pragma unroll
            for (int i = 0; i < 4; ++i) q[i] = ld16(rs, lane * 16 + (i & 1) * 1024, hb + A_Q + stripe_p((TQ + (((i >> 1) ^ kh) * 2)) * 1024, xs));
            const unsigned gk = lane * 16, gv = (A_V - A_K) + h * 256 + (c & 15) * 16, vc = (c >= 16 && (c & 11) == 0) ? 0x3f803f80u : 0u;
            u32x4 kv[9];
#pragma unroll
            for (int i = 0; i < 3; ++i) { kv[3 * i] = ld16(rs, gk + i * 1024, ko + 3 * xs + 5120); kv[3 * i + 1] = u32x4{vc, vc, vc, vc}; kv[3 * i + 2] = u32x4{vc, vc, vc, vc}; }
            if (c < 16) {
#pragma unroll
                for (int i = 0; i < 3; ++i) { kv[3 * i + 1] = ld16(rs, gv + i * 1024, ko + 3 * xs + 5120); kv[3 * i + 2] = ld16(rs, gv + i * 1024 + 512, ko + 3 * xs + 5120); }
            }
            int dl = 4096;
            const u64 wsa = (u64)(uintptr_t)ws, vm = 0x0000FFFF0000FFFFull;
            const u32x4 rsw = {(unsigned)wsa, (unsigned)(wsa >> 32) & 0xffffu, WS_END, 0x00020000u};
            const f32x2 kq = *(LAS f32x2*)(L + T_KN + 128);
            const float bound = __builtin_amdgcn_sqrtf(kq[0] * kq[1]) * 1.001f;
            float mr[4] = {bound, bound, bound, bound};
            if (!__all(bound <= MAX_SAFE)) {
#pragma unroll
                for (int i = 0; i < 4; ++i) mr[i] = -INFINITY;
                u32x4 k0 = ld16(rs, lane * 16, hb + A_K), k1 = ld16(rs, lane * 16 + 1024, hb + A_K);
#pragma unroll 1
                for (int t = 0; t < 64; ++t) {
                    const bf16x8 kf = __builtin_bit_cast(bf16x8, k0);
                    k0 = k1; k1 = ld16(rs, lane * 16, hb + A_K + stripe_p(((t + 2) & 63) * 1024, xs));
#pragma unroll
                    for (int i = 0; i < 4; ++i) {
                        const f32x16 sa = MFMA32(kf, __builtin_bit_cast(bf16x8, q[i]), f32x16{});
#pragma unroll
                        for (int r = 0; r < 16; ++r) mr[i] = fmaxf(mr[i], sa[r]);
                    }
                }
#pragma unroll
                for (int i = 0; i < 4; ++i) mr[i] = swap32_max(mr[i]);
            }
            f32x16 o[4], m[4];
#pragma unroll
            for (int i = 0; i < 4; ++i) {
                o[i] = f32x16{};
#pragma unroll
                for (int r = 0; r < 16; ++r) m[i][r] = -mr[i];
            }
            int idx = 0xDDDD; PIN(idx);
            int n = 8;
            asm volatile(ATTN_LOOP_ASM
                         : [o0] "+&v"(o[0]), [o1] "+&v"(o[1]), [o2] "+&v"(o[2]), [o3] "+&v"(o[3]), [n] "+&s"(n), [ko] "+&s"(ko), [dl] "+&s"(dl), ATTN_LOOP_ASM_KV(kv)
                         : [q0] "v"(q[0]), [q1] "v"(q[1]), [q2] "v"(q[2]), [q3] "v"(q[3]), [m0] "v"(m[0]), [m1] "v"(m[1]), [m2] "v"(m[2]), [m3] "v"(m[3]),
                           [idx] "v"(idx), [gk] "v"(gk), [gv] "v"(gv), [vc] "v"(vc), [vm] "s"(vm), [rs] "s"(rsw), [dx] "s"(xs - 8192)
                         : "memory", "scc", ATTN_LOOP_ASM_CLOB);
            const lds_ptr ex = L + L_EX + lw * 4608, ey = L + L_EX + (lw ^ 2) * 4608;
            *(LAS f32x4*)(ex + lane * 16) = f32x4{o[2][0], o[2][1], o[2][2], o[2][3]};
            *(LAS f32x4*)(ex + 1024 + lane * 16) = f32x4{o[2][4], o[2][5], o[2][6], o[2][7]};
            *(LAS f32x4*)(ex + 2048 + lane * 16) = f32x4{o[3][0], o[3][1], o[3][2], o[3][3]};
            *(LAS f32x4*)(ex + 3072 + lane * 16) = f32x4{o[3][4], o[3][5], o[3][6], o[3][7]};
            *(LAS f32x2*)(ex + 4096 + lane * 8) = f32x2{o[2][8], o[3][8]};
            BAR();
            sides = __builtin_amdgcn_readfirstlane(*(LAS unsigned*)(L + T_KN + 320));
            const f32x4 a0 = *(LAS f32x4*)(ey + lane * 16), a1 = *(LAS f32x4*)(ey + 1024 + lane * 16);
            const f32x4 b0 = *(LAS f32x4*)(ey + 2048 + lane * 16), b1_ = *(LAS f32x4*)(ey + 3072 + lane * 16);
            const f32x2 ll = *(LAS f32x2*)(ey + 4096 + lane * 8);
            const float ia = __builtin_amdgcn_rcpf(o[0][8] + ll[0]), ib = __builtin_amdgcn_rcpf(o[1][8] + ll[1]);
            const unsigned oo = o_off(layer, TQ + kh * 2, sides) + stripe(head * 1024);
            st16(u32x4{pk2((o[0][0] + a0[0]) * ia, (o[0][1] + a0[1]) * ia), pk2((o[0][2] + a0[2]) * ia, (o[0][3] + a0[3]) * ia),
                       pk2((o[0][4] + a1[0]) * ia, (o[0][5] + a1[1]) * ia), pk2((o[0][6] + a1[2]) * ia, (o[0][7] + a1[3]) * ia)}, rs, lane * 16, oo);
            st16(u32x4{pk2((o[1][0] + b0[0]) * ib, (o[1][1] + b0[1]) * ib), pk2((o[1][2] + b0[2]) * ib, (o[1][3] + b0[3]) * ib),
                       pk2((o[1][4] + b1_[0]) * ib, (o[1][5] + b1_[1]) * ib), pk2((o[1][6] + b1_[2]) * ib, (o[1][7] + b1_[3]) * ib)}, rs, lane * 16, oo + 65536);
            GAS u64* op = mbox(ws, e0 + 2, (c << 3) | qb, head); PIN(op);
            DRAIN_BAR();
            if (w == 1 && lane < 32) post(op, tag);
        }
#define WD() WBase wd[2]; \
        wd[0].set(L + kh * 16384 + trq * 128 + trc + ((kh ^ (q4 >> 1)) * 64)); \
        wd[1].set(L + kh * 16384 + trq * 128 + trc + ((kh ^ 1 ^ (q4 >> 1)) * 64))
        auto finish = [&](f32x16 (&acc)[2], int lane) {
            MFMA_DONE2(acc[0], acc[1]);
            const lds_ptr ex = L + L_EX + (w ^ 2) * 4608 + lane * 16;
#pragma unroll
            for (int q = 0; q < 4; ++q) *(LAS f32x4*)(ex + q * 1024) = f32x4{acc[1][4 * q], acc[1][4 * q + 1], acc[1][4 * q + 2], acc[1][4 * q + 3]};
            BAR();
            const lds_ptr ey = L + L_EX + w * 4608 + lane * 16;
#pragma unroll
            for (int q = 0; q < 4; ++q) { const f32x4 v = *(LAS f32x4*)(ey + q * 1024); acc[0][4 * q] += v[0]; acc[0][4 * q + 1] += v[1]; acc[0][4 * q + 2] += v[2]; acc[0][4 * q + 3] += v[3]; }
        };
        {
            COORDS(); WD();
            u32x4 w0[17][2];
#pragma unroll
            for (int s = 0; s < 16; ++s) { w0[s][0] = wfrag(wd[0].lo + (s >> 3) * 32768 + (s & 7) * 2048, 1024); w0[s][1] = wfrag(wd[1].lo + (s >> 3) * 32768 + (s & 7) * 2048, 1024); }
            const unsigned ro = layer == 0 ? WS_X0 + TB * 32768 + stripB * 2048 : x_off(2, TB, sides) + stripe(stripB * 2048);
            const u32x4 r0 = ld16(rs, lane * 16, ro), r1 = ld16(rs, lane * 16 + 1024, ro);
            if (w == 0) wait_tags<0, 1>(mbox(ws, e0 + 2, b, c), tag);
            BAR();
            constexpr int PF = 10;
            const unsigned oo = o_off(layer, TB, sides) + kh * 32768;
            u32x4 af[16];
#pragma unroll
            for (int s = 0; s < PF; ++s) af[s] = ld16(rs, lane * 16 + (s & 3) * 1024, oo + stripe((s >> 2) * 4096));
            f32x16 acc[2];
            gemm_loop<2, 16, 16, 0ull, 16, 1024, false>(acc, af, w0, wd, [](int s) { return (s >> 3) * 32768 + (s & 7) * 2048; },
                [&](int s) { if (s + PF < 16) af[s + PF] = ld16(rs, lane * 16 + ((s + PF) & 3) * 1024, oo + stripe(((s + PF) >> 2) * 4096)); }, [](int, int) {});
            finish(acc, lane);
            unsigned xr[8];
#pragma unroll
            for (int j = 0; j < 8; ++j) { const unsigned ru = j < 4 ? r0[j & 3] : r1[j & 3]; xr[j] = pk2(acc[0][2 * j] + bflo(ru), acc[0][2 * j + 1] + bfhi(ru)); }
            emit_x(rs, x_off(layer * 2 + 1, TB, sides) + stripe(stripB * 2048), st_off(layer * 2 + 1, TB, sides) + stripB * 256, lane, xr);
            GAS u64* xp = mbox(ws, e0 + 3, xcons, cbA); PIN(xp);
            DRAIN_BAR();
            if (w == 1 && lane < 16) post(xp, tag);
        }
        if (((blockIdx.x >> 3) & 7) < 7) {
            COORDS();
            WBase wb[2]; u32x4 w0[3][2];
#pragma unroll
            for (int a = 0; a < 2; ++a) { wb[a].set(L + trq * 128 + trc + ((a ^ (q4 >> 1)) * 64)); w0[0][a] = wfrag(wb[a].hi, 1024); w0[1][a] = wfrag(wb[a].hi + 2048, 1024); }
            if (w == 0) wait_tags<8, 2>(mbox(ws, e0 + 3, b, l16), tag);
            BAR_RAW();
            constexpr int PF = 12;
            const unsigned xo = x_off(layer * 2 + 1, TA, sides);
            u32x4 xf[32];
#pragma unroll
            for (int s = 0; s < PF; ++s) xf[s] = ld16(rs, lane * 16 + (s & 3) * 1024, xo + stripe((s >> 2) * 4096));
            u32x2 sp[8];
            f32x16 acc[4];
            u32x4 ff[2], tf;
            float c1, c3, isc;
            constexpr float GA = -2.0f * 0.7978845608028654f * 1.4426950408889634f, GB = GA * 0.044715f;
            gemm_loop<2, 32, 2, 0ull, 32, 1024, true>(acc, xf, w0, wb, [](int s) { return (((s >> 4) + 2) & 3) * 32768 + (s & 15) * 2048; },
                [&](int s) {
                    if (s + PF < 32) xf[s + PF] = ld16(rs, lane * 16 + ((s + PF) & 3) * 1024, xo + stripe(((s + PF) >> 2) * 4096));
                    if (s >= 20 && s < 28) sp[s - 20] = ld8(rs, h * 2048 + c * 8 + (s - 20) * 256, st_off(layer * 2 + 1, TA, sides));
                    if (s == 30) {
                        float mean, var; row_stats(sp, mean, var);
                        isc = __builtin_amdgcn_sqrtf(fmaf(var + LN_EPS, LN_EPS, var));
                        const float sc = __builtin_amdgcn_rcpf(isc);
                        c1 = GA * sc; c3 = GB * sc * sc * sc; tf = tok_frag(mean, isc, h);
                        PIN(c1); PIN(c3); PIN(isc); PIN(tf);
                        ff[0] = *(LAS u32x4*)(L + T_FU + c * 16); ff[1] = *(LAS u32x4*)(L + T_FU + 512 + c * 16);
                    }
                }, [](int, int) {});
            MFMA_ASM(acc[0], ff[0], tf); MFMA_ASM(acc[1], ff[1], tf);
            const unsigned ho = h_off(layer, TA, sides) + cbU * 16384;
            {
                float t0, t1;
                unsigned pk[4];
                auto gelu2 = [&](int e, int ph) {
                    if (ph == 0)
                        asm volatile("v_mul_f32 %0, %2, %2\n\tv_mul_f32 %1, %3, %3\n\tv_fma_f32 %0, %0, %4, %5\n\tv_fma_f32 %1, %1, %4, %5\n\tv_mul_f32 %0, %0, %2\n\tv_mul_f32 %1, %1, %3"
                                     : "=&v"(t0), "=&v"(t1) : "v"(acc[e >> 4][e & 15]), "v"(acc[e >> 4][(e & 15) + 1]), "v"(c3), "v"(c1));
                    else if (ph == 1)
                        asm volatile("v_exp_f32 %0, %0\n\tv_exp_f32 %1, %1\n\tv_fma_f32 %0, %0, %2, %2\n\tv_fma_f32 %1, %1, %2, %2" : "+v"(t0), "+v"(t1) : "v"(isc));
                    else
                        asm volatile("v_rcp_f32 %1, %1\n\tv_rcp_f32 %2, %2\n\tv_mul_f32 %1, %1, %3\n\tv_mul_f32 %2, %2, %4\n\tv_cvt_pk_bf16_f32 %0, %1, %2"
                                     : "=v"(pk[(e >> 1) & 3]), "+v"(t0), "+v"(t1) : "v"(acc[e >> 4][e & 15]), "v"(acc[e >> 4][(e & 15) + 1]));
                };
                gemm_loop<2, 32, 2, 1ull << 1, 32, 1024, false>(acc + 2, xf, w0, wb, [](int s) { return ((s >> 4) & 3) * 32768 + (s & 15) * 2048; },
                    [&](int s) {
                        if (s >= 7 && s <= 25 && (s - 7) % 6 == 0) st16(u32x4{pk[0], pk[1], pk[2], pk[3]}, rs, lane * 16 + ((s - 7) / 6) * 1024, ho);
                        if (s == 30) { ff[0] = *(LAS u32x4*)(L + T_FU + 1024 + c * 16); ff[1] = *(LAS u32x4*)(L + T_FU + 1536 + c * 16); }
                    },
                    [&](int s, int a) { const int g = s * 2 + a - 4; if (g >= 0 && g < 48) gelu2((g / 3) * 2, g % 3); });
                MFMA_ASM(acc[2], ff[0], tf);
                MFMA_ASM(acc[3], ff[1], tf);
            }
            DRAIN_BAR();
            MFMA_DONE2(acc[2], acc[3]);
            f32x16 t[2];
#pragma unroll
            for (int a = 0; a < 2; ++a) { t[a] = acc[2 + a] * acc[2 + a]; PIN(t[a]); }
#pragma unroll
            for (int a = 0; a < 2; ++a) { t[a] = t[a] * c3 + c1; PIN(t[a]); }
#pragma unroll
            for (int a = 0; a < 2; ++a) { t[a] = t[a] * acc[2 + a]; PIN(t[a]); }
#pragma unroll
            for (int a = 0; a < 2; ++a) {
#pragma unroll
                for (int r = 0; r < 16; ++r) t[a][r] = __builtin_amdgcn_exp2f(t[a][r]);
                PIN(t[a]);
                t[a] = t[a] * isc + isc; PIN(t[a]);
#pragma unroll
                for (int r = 0; r < 16; ++r) t[a][r] = __builtin_amdgcn_rcpf(t[a][r]);
                PIN(t[a]);
                t[a] = t[a] * acc[2 + a]; PIN(t[a]);
                const u32x4 y0 = {pk2(t[a][0], t[a][1]), pk2(t[a][2], t[a][3]), pk2(t[a][4], t[a][5]), pk2(t[a][6], t[a][7])};
                const u32x4 y1 = {pk2(t[a][8], t[a][9]), pk2(t[a][10], t[a][11]), pk2(t[a][12], t[a][13]), pk2(t[a][14], t[a][15])};
                st16(y0, rs, lane * 16, ho + 4096 + a * 2048);
                st16(y1, rs, lane * 16 + 1024, ho + 4096 + a * 2048);
                KEEP2(y0, y1);
            }
        } else {
            BAR_RAW(); BAR_RAW(); BAR_RAW();
        }
        {
            COORDS(); WD();
            u32x2 bv[4];
            u32x4 rr[2];
            const rsrc_t rb = __builtin_amdgcn_make_buffer_rsrc((void*)b2, 0, 1024, 0x00020000);
            u32x4 w0[17][2];
#pragma unroll
            for (int s = 0; s < 16; ++s) { w0[s][0] = wfrag(wd[0].hi + (s >> 3) * 32768 + (s & 7) * 2048, 1024); w0[s][1] = wfrag(wd[1].hi + (s >> 3) * 32768 + (s & 7) * 2048, 1024); }
            DRAIN_BAR();
            constexpr int PF = 8;
            const unsigned ho = h_off(layer, TB, sides) + kh * (7 * 16384);
            auto ldh = [&](int s) { return ld16(rs, lane * 16 + (s & 3) * 1024, ho + (s >= 28 ? ((s - 28) >> 2) * 16384 + 4096 : (s >> 2) * 16384)); };
            u32x4 af[PF];
#pragma unroll
            for (int s = 0; s < PF; ++s) af[s] = ldh(s);
            f32x16 acc[2];
            gemm_loop<2, 56, 16, (1ull << 25) | (1ull << 41) | (1ull << (27 - PF)), PF, 1024, false>(acc, af, w0, wd, [](int s) { return (((s >> 3) + 2) & 3) * 32768 + (s & 7) * 2048; },
                [&](int s) {
                    if (s + PF < 56) af[s % PF] = ldh(s + PF);
                    if (s >= 48 && s < 52) bv[s - 48] = __builtin_amdgcn_raw_buffer_load_b64(rb, h * 8 + (s - 48) * 16, stripB * 64, 0);
                    if (s >= 52 && s < 54) rr[s - 52] = ld16(rs, lane * 16 + (s - 52) * 1024, x_off(layer * 2 + 1, TB, sides) + stripe(stripB * 2048));
                }, [](int, int) {});
            finish(acc, lane);
            unsigned xr[8];
#pragma unroll
            for (int j = 0; j < 8; ++j) {
                const unsigned bu = bv[j >> 1][j & 1], ru = rr[j >> 2][j & 3];
                xr[j] = pk2(acc[0][2 * j] + (bflo(bu) + bflo(ru)), acc[0][2 * j + 1] + (bfhi(bu) + bfhi(ru)));
            }
            if (layer == 0) {
                emit_x(rs, x_off(2, TB, sides) + stripe(stripB * 2048), st_off(2, TB, sides) + stripB * 256, lane, xr);
                GAS u64* xp = mbox(ws, 5, xcons, cbA); PIN(xp);
                DRAIN_BAR();
                if (w == 1 && lane < 16) post(xp, tag);
            } else {
                const lds_ptr tb = L + w * 2560;
#pragma unroll
                for (int q = 0; q < 4; ++q) *(LAS u32x2*)(tb + c * 80 + q * 16 + h * 8) = u32x2{xr[2 * q], xr[2 * q + 1]};
                const u32x4 v0 = *(LAS u32x4*)(tb + (lane >> 2) * 80 + (lane & 3) * 16), v1 = *(LAS u32x4*)(tb + (16 + (lane >> 2)) * 80 + (lane & 3) * 16);
                const rsrc_t ro = __builtin_amdgcn_make_buffer_rsrc((void*)out, 0, 2 * MB, 0x00020000);
                __builtin_amdgcn_raw_buffer_store_b128(v0, ro, (lane >> 2) * 1024 + (lane & 3) * 16, TB * 32768 + stripB * 64, 0);
                __builtin_amdgcn_raw_buffer_store_b128(v1, ro, (lane >> 2) * 1024 + (lane & 3) * 16, TB * 32768 + 16384 + stripB * 64, 0);
                KEEP2(v0, v1);
                if (b == 0 && w == 0 && lane == 0) post((GAS u64*)(ws + WS_EPOCH), epn);
            }
        }
    }
}

extern "C" void kernel_launch(void* const* d_in, const int* in_sizes, int n_in, void* d_out, int out_size, void* d_ws, size_t ws_size,
                              hipStream_t stream) {
    char* ws = (char*)(((uintptr_t)d_ws + 2 * MB - 1) & ~(uintptr_t)(2 * MB - 1));
    (void)hipFuncSetAttribute((const void*)k_all, hipFuncAttributeMaxDynamicSharedMemorySize, LDS_BYTES);
    k_all<<<256, 512, LDS_BYTES, stream>>>((const u16*)d_in[0], (const u16*)d_in[1], (const u16*)d_in[2], (const u16*)d_in[4], (const u16*)d_in[5],
                                                        (const u16*)d_in[6], (const u16*)d_in[7], (u16*)d_out, ws);
}
```
